```python
import torch

def _softmax(x, axis=-1):
    return torch.softmax(x, dim=axis)
def _gelu(x):
    return torch.nn.functional.gelu(x, approximate="tanh")
def _silu(x):
    return torch.nn.functional.silu(x)
def _sigmoid(x):
    return torch.sigmoid(x)
def _layernorm(x, axis=-1, eps=1e-5):
    m = x.mean(dim=axis, keepdim=True)
    v = x.var(dim=axis, keepdim=True, unbiased=False)
    return (x - m) / torch.sqrt(v + eps)
def _scatter_add(tgt, idx, src, axis=0):
    return tgt.clone().index_add_(axis, idx, src)
def _segment_sum(src, seg, n):
    out = torch.zeros((n,) + src.shape[1:], dtype=src.dtype)
    return out.index_add_(0, seg, src)
def _pad(x, pw):
    flat = tuple(v for pair in reversed(pw) for v in pair)
    return torch.nn.functional.pad(x, flat)

kernel_name = 'interleave_layers__sliding_block__mha_block__prompt'

def setup_inputs():

    _w = (lambda a: (lambda t: t.to(torch.bfloat16) if t.is_floating_point() else t)(torch.as_tensor(a)))
    def _A_setup():
        B, S, D = 1, 2048, 640
        return {
            "x": _w((lambda *s: torch.randn(*s, dtype=torch.bfloat16) * 0.05)(B, S, D)),
            "wqkv": _w((lambda *s: torch.randn(*s, dtype=torch.bfloat16) * 0.05)(D, 3 * D)),
            "wo": _w((lambda *s: torch.randn(*s, dtype=torch.bfloat16) * 0.05)(D, D)),
        }
    _A_in = _A_setup()

    def _B_setup():
        B, S, D, H = 1, 2048, 640, 32
        return {
            "x": _w((lambda *s: torch.randn(*s, dtype=torch.bfloat16) * 0.05)(B, S, D)),
            "wqkv": _w((lambda *s: torch.randn(*s, dtype=torch.bfloat16) * 0.05)(D, 3 * D)),
            "wo": _w((lambda *s: torch.randn(*s, dtype=torch.bfloat16) * 0.05)(D, D)),
        }
    _B_in = _B_setup()

    return {
        "A_x": _A_in["x"],
        "A_wqkv": _A_in["wqkv"],
        "A_wo": _A_in["wo"],
        "B_x": _B_in["x"],
        "B_wqkv": _B_in["wqkv"],
        "B_wo": _B_in["wo"],
    }

def reference(A_x, A_wqkv, A_wo, B_x, B_wqkv, B_wo):
    def _A_ref(x, wqkv, wo):
        B, S, D = x.shape
        H = 32; Dh = D // H; W = 256
        qkv = (lambda x, *d: x.permute(*d))((x @ wqkv).reshape(B, S, 3, H, Dh), 2, 0, 3, 1, 4)
        q, k, v = qkv[0], qkv[1], qkv[2]
        scores = torch.einsum("bhqd,bhkd->bhqk", q, k) * (Dh ** -0.5)
        idx = torch.arange(S)
        mask = (idx[:, None] - idx[None, :] >= 0) & (idx[:, None] - idx[None, :] < W)
        scores = torch.where(mask[None, None], scores, -1e9)
        p = _softmax(scores, axis=-1)
        o = torch.einsum("bhqk,bhkd->bhqd", p, v)
        return (lambda x, *d: x.permute(*d))(o, 0, 2, 1, 3).reshape(B, S, D) @ wo

    def _B_ref(x, wqkv, wo):
        B, S, D = x.shape
        H = 32
        Dh = D // H
        qkv = (lambda x, *d: x.permute(*d))((x @ wqkv).reshape(B, S, 3, H, Dh), 2, 0, 3, 1, 4)
        q, k, v = qkv[0], qkv[1], qkv[2]
        scores = torch.einsum("bhqd,bhkd->bhqk", q, k) * (Dh ** -0.5)
        p = _softmax(scores, axis=-1)
        o = torch.einsum("bhqk,bhkd->bhqd", p, v)
        return (lambda x, *d: x.permute(*d))(o, 0, 2, 1, 3).reshape(B, S, D) @ wo

    x = A_x
    x = x + _A_ref(_layernorm(x, axis=-1), A_wqkv, A_wo)
    x = x + _B_ref(_layernorm(x, axis=-1), B_wqkv, B_wo)
    x = x + _A_ref(_layernorm(x, axis=-1), A_wqkv, A_wo)
    x = x + _B_ref(_layernorm(x, axis=-1), B_wqkv, B_wo)
    return x
```

```cpp
#include <hip/hip_runtime.h>
#include <hip/hip_bf16.h>

typedef float f32x16 __attribute__((ext_vector_type(16)));
typedef float f32x8 __attribute__((ext_vector_type(8)));
typedef float f32x4 __attribute__((ext_vector_type(4)));
typedef unsigned u32x4 __attribute__((ext_vector_type(4)));
typedef unsigned u32x2 __attribute__((ext_vector_type(2)));
typedef __bf16 bf16x16 __attribute__((ext_vector_type(16)));
typedef __bf16 bf16x8 __attribute__((ext_vector_type(8)));
typedef __bf16 bf16x2 __attribute__((ext_vector_type(2)));
typedef short s16x4 __attribute__((ext_vector_type(4)));
typedef unsigned short u16;

#define LAS __attribute__((address_space(3)))
#define GAS __attribute__((address_space(1)))
typedef GAS unsigned gu32;
#define RLX_AGENT __ATOMIC_RELAXED, __HIP_MEMORY_SCOPE_AGENT
#define DEVI __device__ __forceinline__

constexpr unsigned WQ_HEAD = 81920;
constexpr unsigned WO_CG = 102400;
constexpr unsigned XIMG = 2621440;
constexpr unsigned KV_K = 12288, KV_V = 10752, KV_CHUNK = KV_K + KV_V;
constexpr unsigned KV_LAYER = 32 * 8 * KV_CHUNK;
constexpr unsigned OFF_FLAG = 0;
constexpr unsigned FLAG_BYTES = 65536;
constexpr unsigned OFF_WQ = FLAG_BYTES;
constexpr unsigned OFF_WO = OFF_WQ + 2 * 32 * WQ_HEAD;
constexpr unsigned OFF_CS = OFF_WO + 2 * 8 * WO_CG;
constexpr unsigned OFF_X = OFF_CS + 2 * 10 * 1920 * 4;
constexpr unsigned OFF_O = OFF_X + 4 * XIMG;
constexpr unsigned OFF_KV = OFF_O + 4 * XIMG;
constexpr unsigned WS_USED = OFF_KV + 4 * KV_LAYER;
constexpr unsigned OFF_DBG = WS_USED;
constexpr unsigned FL_KV = 0;
constexpr unsigned FL_O = 4 * 32 * 32;
constexpr unsigned FL_X = FL_O + 4 * 8 * 32;
constexpr unsigned FL_TMO = FL_X + 4 * 8 * 32;

constexpr unsigned L_A = 0;
constexpr unsigned L_K = 102400;
constexpr unsigned L_V = L_K + 2 * KV_K;
constexpr unsigned L_M = L_V + 3 * KV_V;
constexpr unsigned LDS_BYTES = 163840;
constexpr int KS_B = 27;

constexpr unsigned kSpinCap = 1u << 18;
#define NODOT 0
#define ABL 0
#define XPD 10
#define MAPC 2
#define SUM_LIMIT 1.0e18f

DEVI unsigned pack2(float a, float b) {
  bf16x2 v = {(__bf16)a, (__bf16)b};
  return __builtin_bit_cast(unsigned, v);
}
DEVI float sq2(unsigned u, float acc) {
  const bf16x2 p = __builtin_bit_cast(bf16x2, u);
  return __builtin_amdgcn_fdot2_f32_bf16(p, p, acc, false);
}
DEVI float bf_lo(unsigned u) { return __uint_as_float(u << 16); }
DEVI float bf_hi(unsigned u) { return __uint_as_float(u & 0xffff0000u); }
DEVI f32x16 mfma32(u32x4 a, u32x4 b, f32x16 c) {
  return __builtin_amdgcn_mfma_f32_32x32x16_bf16(__builtin_bit_cast(bf16x8, a), __builtin_bit_cast(bf16x8, b), c, 0, 0, 0);
}
DEVI f32x16 mfma32k8(u32x2 a, u32x2 b, f32x16 c) {
  return __builtin_amdgcn_mfma_f32_32x32x8bf16_1k(__builtin_bit_cast(s16x4, a), __builtin_bit_cast(s16x4, b), c, 0, 0, 0);
}
DEVI f32x4 mfma16(u32x4 a, u32x4 b, f32x4 c) {
  return __builtin_amdgcn_mfma_f32_16x16x32_bf16(__builtin_bit_cast(bf16x8, a), __builtin_bit_cast(bf16x8, b), c, 0, 0, 0);
}
DEVI f32x16 pv_lo(u32x4 v, const f32x16& p, f32x16 o) {
  f32x8 h = __builtin_shufflevector(p, p, 0, 1, 2, 3, 4, 5, 6, 7);
  return __builtin_amdgcn_smfmac_f32_32x32x32_bf16(__builtin_bit_cast(bf16x8, v), __builtin_bit_cast(bf16x16, h), o, 0xDDDD, 0, 0);
}
DEVI f32x16 pv_hi(u32x4 v, const f32x16& p, f32x16 o) {
  f32x8 h = __builtin_shufflevector(p, p, 8, 9, 10, 11, 12, 13, 14, 15);
  return __builtin_amdgcn_smfmac_f32_32x32x32_bf16(__builtin_bit_cast(bf16x8, v), __builtin_bit_cast(bf16x16, h), o, 0xDDDD, 0, 0);
}
DEVI u32x4 lds_r128(unsigned a) { return *(LAS u32x4*)a; }
DEVI u32x2 lds_r64(unsigned a) { return *(LAS u32x2*)a; }
DEVI void lds_w128(unsigned a, u32x4 v) { *(LAS u32x4*)a = v; }
DEVI void lds_w64(unsigned a, u32x2 v) { *(LAS u32x2*)a = v; }
DEVI void lds_w16(unsigned a, float v) { *(LAS __bf16*)a = (__bf16)v; }
DEVI float xor32(float v) { return __shfl_xor(v, 32); }
#define SB() __builtin_amdgcn_sched_barrier(0)
DEVI void bar_lds() {
  asm volatile("s_waitcnt lgkmcnt(0)\n\ts_barrier" ::: "memory");
}
DEVI void drain_vm() { asm volatile("s_waitcnt vmcnt(0)" ::: "memory"); }
DEVI void dma1k(unsigned lds_dst, const char* gsrc) {
  unsigned keep;
  asm volatile("s_mov_b32 %0, m0\n\ts_mov_b32 m0, %2\n\ts_nop 0\n\tglobal_load_lds_dwordx4 %1, off\n\ts_mov_b32 m0, %0"
               : "=&s"(keep) : "v"(gsrc), "s"(lds_dst) : "memory");
}
typedef __amdgpu_buffer_rsrc_t rsrc_t;
DEVI u32x4 ld128(rsrc_t r, unsigned voff, unsigned soff) { return __builtin_amdgcn_raw_buffer_load_b128(r, voff, soff, 16); }
DEVI u32x2 ld64(rsrc_t r, unsigned voff, unsigned soff) { return __builtin_amdgcn_raw_buffer_load_b64(r, voff, soff, 16); }
DEVI void st128(u32x4 v, rsrc_t r, unsigned voff, unsigned soff) { __builtin_amdgcn_raw_buffer_store_b128(v, r, voff, soff, 16); }
DEVI void st64(u32x2 v, rsrc_t r, unsigned voff, unsigned soff) { __builtin_amdgcn_raw_buffer_store_b64(v, r, voff, soff, 16); }

DEVI void wait_flags(gu32* words, int n, int lane, gu32* tmo) {
  for (unsigned spins = 0;;) {
    unsigned v = 1;
    if (lane < n) v = __hip_atomic_load(words + lane, RLX_AGENT);
    if (__builtin_amdgcn_ballot_w64(v == 0) == 0) break;
    __builtin_amdgcn_s_sleep(1);
    if (++spins > kSpinCap) { if (lane == 0) __hip_atomic_store(tmo, 1u, RLX_AGENT); break; }
    if ((spins & 255u) == 0u && __builtin_amdgcn_readfirstlane(__hip_atomic_load(tmo, RLX_AGENT))) break;
  }
  __builtin_amdgcn_fence(__ATOMIC_ACQUIRE, "wavefront");
}

#define STAMP(i) do {} while (0)

constexpr int PB_WQ = 2 * 10 * 8;
constexpr int PB_WO = 2 * 10 * 3;
constexpr int PB_X = 320;
constexpr int PB_TOTAL = PB_WQ + PB_WO + PB_X + 1;

DEVI void transpose8x4(const u32x2 (&r)[8], u32x4 (&o)[4]) {
#pragma unroll
  for (int p = 0; p < 4; ++p) {
    o[0][p] = __builtin_amdgcn_perm(r[2 * p + 1].x, r[2 * p].x, 0x05040100u);
    o[1][p] = __builtin_amdgcn_perm(r[2 * p + 1].x, r[2 * p].x, 0x07060302u);
    o[2][p] = __builtin_amdgcn_perm(r[2 * p + 1].y, r[2 * p].y, 0x05040100u);
    o[3][p] = __builtin_amdgcn_perm(r[2 * p + 1].y, r[2 * p].y, 0x07060302u);
  }
}

__global__ void __launch_bounds__(512) setup_kernel(const u16* __restrict__ x, const u16* __restrict__ wqA, const u16* __restrict__ woA,
                                                    const u16* __restrict__ wqB, const u16* __restrict__ woB, char* __restrict__ ws) {
  __shared__ float red[8][256];
  const int tid = threadIdx.x, lane = tid & 63, w = tid >> 6;
  int b = blockIdx.x;
  if (b < PB_WQ) {
    const int m = b / 80, kb = (b % 80) / 8, cq = (b % 8) * 64 + lane;
    const int kh = kb * 8 + w;
    const u16* W = m ? wqB : wqA;
    float s[4] = {0.f, 0.f, 0.f, 0.f};
    if (cq < 480) {
      u32x2 r[8];
#pragma unroll
      for (int j = 0; j < 8; ++j) r[j] = *(const u32x2*)(W + (size_t)(kh * 8 + j) * 1920 + cq * 4);
      u32x4 o[4];
      transpose8x4(r, o);
#pragma unroll
      for (int j = 0; j < 8; ++j) { s[0] += bf_lo(r[j].x); s[1] += bf_hi(r[j].x); s[2] += bf_lo(r[j].y); s[3] += bf_hi(r[j].y); }
      const int which = cq / 160, h = (cq % 160) / 5, d = (cq % 5) * 4;
      int mt, row;
      if (which == 0) { mt = 0; row = d; }
      else if (which == 1) { mt = 1; row = d; }
      else if (d < 12) { mt = 0; row = 20 + d; }
      else { mt = 1; row = 8 + d; }
      char* dst = ws + OFF_WQ + (size_t)(m * 32 + h) * WQ_HEAD + (kh >> 1) * 2048 + (kh & 1) * 512;
      u32x4* p = (u32x4*)(dst + mt * 1024 + row * 16);
#pragma unroll
      for (int i = 0; i < 4; ++i) p[i] = o[i];
      if (which == 2 && d == 16) {
        const u32x4 one = {0x3F803F80u, 0x3F803F80u, 0x3F803F80u, 0x3F803F80u}, zero = {0u, 0u, 0u, 0u};
        p[4] = one; p[5] = zero; p[6] = zero; p[7] = zero;
      }
    }
#pragma unroll
    for (int i = 0; i < 4; ++i) red[w][lane * 4 + i] = s[i];
    __syncthreads();
    if (tid < 256) {
      float t = 0.f;
#pragma unroll
      for (int i = 0; i < 8; ++i) t += red[i][tid];
      const int col = (b % 8) * 256 + tid;
      if (col < 1920) ((float*)(ws + OFF_CS))[(m * 10 + kb) * 1920 + col] = t;
    }
    return;
  }
  b -= PB_WQ;
  if (b < PB_WO) {
    const int m = b / 30, q = ((b % 30) / 3) * 8 + w, cq = (b % 3) * 64 + lane;
    if (cq >= 160) return;
    const u16* W = m ? woB : woA;
    int k0, k1;
    if (q < 64) { k0 = (q >> 1) * 20 + (q & 1) * 4; k1 = k0 + 8; }
    else { k0 = (q - 64) * 40 + 16; k1 = k0 + 20; }
    u32x2 r[8];
#pragma unroll
    for (int j = 0; j < 4; ++j) {
      r[j] = *(const u32x2*)(W + (size_t)(k0 + j) * 640 + cq * 4);
      r[4 + j] = *(const u32x2*)(W + (size_t)(k1 + j) * 640 + cq * 4);
    }
    u32x4 o[4];
    transpose8x4(r, o);
    const int cg = cq / 20, c4 = cq % 20;
    int nt, g;
    if (c4 < 16) { nt = (c4 >> 3) * 2 + (c4 & 1); g = (c4 >> 1) & 3; }
    else { nt = 4; g = c4 - 16; }
    char* dst = ws + OFF_WO + (size_t)(m * 8 + cg) * WO_CG + ((q >> 2) * 5 + nt) * 1024 + (q & 3) * 256 + g * 64;
#pragma unroll
    for (int i = 0; i < 4; ++i) ((u32x4*)dst)[i] = o[i];
    return;
  }
  b -= PB_WO;
  if (b < PB_X) {
    const int unit = b * 8 + w;
    const int row = (unit / 10) * 8 + (lane & 7), ch = (unit % 10) * 8 + (lane >> 3);
    const u32x4 v = *(const u32x4*)(x + (size_t)row * 640 + ch * 8);
    *(u32x4*)(ws + OFF_X + (row >> 5) * 40960 + ch * 512 + (row & 31) * 16) = v;
    return;
  }
  const u32x4 zero = {0u, 0u, 0u, 0u};
  for (int i = tid; i < (int)(FLAG_BYTES / 16); i += 512) ((u32x4*)(ws + OFF_FLAG))[i] = zero;
}

struct Frag { u32x4 k1; u32x2 k2; u32x4 va, vb; };

template <bool QK, bool LDK, bool LDV>
DEVI void att_step(f32x16& cur, f32x16& oth, f32x16& o, Frag& f, const u32x4& q1, const u32x2& q2, const f32x16& cin,
                   unsigned a_vb, unsigned a_k1, unsigned a_k2, unsigned a_va) {
  o = pv_hi(f.vb, oth, o);
  f.vb = lds_r128(a_vb);
  SB();
#pragma unroll
  for (int i = 0; i < 4; ++i) cur[i] = __builtin_amdgcn_exp2f(cur[i]);
  SB();
  if (QK) oth = mfma32(f.k1, q1, cin);
  if (LDK) f.k1 = lds_r128(a_k1);
  SB();
#pragma unroll
  for (int i = 4; i < 8; ++i) cur[i] = __builtin_amdgcn_exp2f(cur[i]);
  SB();
  if (QK) oth = mfma32k8(f.k2, q2, oth);
  if (LDK) f.k2 = lds_r64(a_k2);
  SB();
#pragma unroll
  for (int i = 8; i < 12; ++i) cur[i] = __builtin_amdgcn_exp2f(cur[i]);
  SB();
  o = pv_lo(f.va, cur, o);
  if (LDV) f.va = lds_r128(a_va);
  SB();
#pragma unroll
  for (int i = 12; i < 16; ++i) cur[i] = __builtin_amdgcn_exp2f(cur[i]);
  SB();
}
DEVI void bar_if_eq(int a, int b) {
  asm volatile("s_cmp_lg_u32 %0, %1\n\ts_cbranch_scc1 .Lskip%=\n\ts_waitcnt lgkmcnt(0)\n\ts_barrier\n.Lskip%=:" :: "s"(a), "s"(b) : "memory", "scc");
}
DEVI int opaque(int v) { asm volatile("" : "+v"(v)); return v; }

DEVI float max16(const f32x16& s) {
  float m = fmaxf(fmaxf(s[0], s[1]), s[2]);
#pragma unroll
  for (int i = 3; i < 15; i += 2) m = fmaxf(fmaxf(m, s[i]), s[i + 1]);
  return fmaxf(m, s[15]);
}
DEVI void set_shift(u32x2& q2, float m, int hh) {
  const unsigned hi = __float_as_uint(m) & 0xffff0000u;
  const float r1 = m - __uint_as_float(hi);
  const unsigned mid = __float_as_uint(r1) & 0xffff0000u;
  const float r2 = r1 - __uint_as_float(mid);
  const unsigned lo = __float_as_uint(r2) & 0xffff0000u;
  if (hh) { q2.x = ((hi >> 16) | mid) ^ 0x80008000u; q2.y = ((lo >> 16) ^ 0x8000u) | 0xFF7F0000u; }
}
DEVI f32x16 tri_mask(int t, int hh, bool diag) {
  f32x16 c;
#pragma unroll
  for (int i = 0; i < 16; ++i) {
    const int key = (i & 3) + 8 * (i >> 2) + 4 * hh;
    c[i] = ((key <= t) == diag) ? 0.f : -3.0e38f;
  }
  return c;
}

__global__ void __launch_bounds__(512, 2) main_kernel(char* __restrict__ ws, u16* __restrict__ out) {
  extern __shared__ __attribute__((aligned(16))) char smem[];
  const int tid0 = threadIdx.x;
  const int w = __builtin_amdgcn_readfirstlane(tid0 >> 6);
  const int tt = w < 4 ? w : 11 - w;
  const int xg = blockIdx.x & 7, xi = blockIdx.x >> 3;
  const int c = ((xg << MAPC) & 7) | (xi & ((1 << MAPC) - 1));
  const int h = ((xg >> (3 - MAPC)) << (5 - MAPC)) | (xi >> MAPC);
#define LANE_VARS const int tid = opaque(tid0), lane = tid & 63, t = lane & 31, hh = lane >> 5; (void)t; (void)hh
  gu32* flags = (gu32*)(ws + OFF_FLAG);
  gu32* tmo = flags + FL_TMO;
  const rsrc_t rs = __builtin_amdgcn_make_buffer_rsrc(ws, 0, WS_USED, 0x00020000);
  const rsrc_t ro = __builtin_amdgcn_make_buffer_rsrc(out, 0, XIMG, 0x00020000);
  const f32x16 zero16 = {0.f, 0.f, 0.f, 0.f, 0.f, 0.f, 0.f, 0.f, 0.f, 0.f, 0.f, 0.f, 0.f, 0.f, 0.f, 0.f};
  STAMP(0);

  {
    LANE_VARS;
    const char* src = ws + OFF_WQ + (size_t)h * WQ_HEAD + lane * 16;
    for (int i = w; i < 80; i += 8) dma1k(i < 2 * KS_B ? L_K + i * 1024 : L_A + (i - 2 * KS_B) * 1024, src + i * 1024);
    if (tid < 128) {
      const int m = tid >> 6, mt = (tid >> 5) & 1, r = tid & 31;
      int col = -1;
      if (r < 20) col = mt * 640 + h * 20 + r;
      else if (mt == 0) col = 1280 + h * 20 + r - 20;
      else if (r < 28) col = 1280 + h * 20 + r - 8;
      float s = 0.f;
      if (col >= 0) {
#pragma unroll
        for (int i = 0; i < 10; ++i) s += ((const float*)(ws + OFF_CS))[(m * 10 + i) * 1920 + col];
      }
      *(LAS float*)(L_M + tid * 4) = s;
    }
    if (tid == 0) *(LAS unsigned*)(L_M + 512) = 0u;
    drain_vm();
    bar_lds();
  }

  for (int layer = 0; layer < 4; ++layer) {
    const int m = layer & 1;
    u32x4 q1; u32x2 q2;
    {
      LANE_VARS;
      if (layer > 0) wait_flags(flags + FL_X + (layer * 8 + c) * 32 + (tt >> 1) * 8, 8, lane, tmo);
      STAMP(1 + layer * 10);
      const unsigned xoff = OFF_X + layer * XIMG + (c * 8 + tt) * 40960;
      constexpr int PD = XPD;
      u32x4 xb[40];
#pragma unroll
      for (int ks = 0; ks < PD; ++ks) xb[ks] = ld128(rs, lane * 16 + (ks & 3) * 1024, xoff + (ks >> 2) * 4096);
      SB();
      f32x16 a0 = zero16, a1 = zero16;
      float ss = 0.f;
      const unsigned wb = opaque(L_K + lane * 16), wa = opaque(L_A + lane * 16);
      u32x4 wf[3][2];
#pragma unroll
      for (int ks = 0; ks < 2; ++ks) { wf[ks][0] = lds_r128(wb + ks * 2048); wf[ks][1] = lds_r128(wb + ks * 2048 + 1024); }
#pragma unroll
      for (int ks = 0; ks < 40; ++ks) {
        const int kn = ks + 2, kx = ks + PD;
        if (kn == KS_B) bar_lds();
        if (kn < 40) {
          const unsigned ad = kn < KS_B ? wb + kn * 2048 : wa + (kn - KS_B) * 2048;
          wf[kn % 3][0] = lds_r128(ad); wf[kn % 3][1] = lds_r128(ad + 1024);
        }
        SB();
        a0 = mfma32(wf[ks % 3][0], xb[ks], a0);
        if (!NODOT) { ss = sq2(xb[ks].x, ss); ss = sq2(xb[ks].y, ss); }
        SB();
        a1 = mfma32(wf[ks % 3][1], xb[ks], a1);
        if (!NODOT) { ss = sq2(xb[ks].z, ss); ss = sq2(xb[ks].w, ss); }
        if (kx < 40) xb[kx] = ld128(rs, lane * 16 + (kx & 3) * 1024, xoff + (kx >> 2) * 4096);
        SB();
      }
      STAMP(10 + layer * 10);
      const float sx = xor32(a1[12]);
      const float sum = hh ? a1[12] : sx;
      ss += xor32(ss);
      const float mean = sum * (1.f / 640.f);
      const float var = fmaxf(ss * (1.f / 640.f) - mean * mean, 0.f);
      const float rstd = __builtin_amdgcn_rsqf(var + 1e-5f);
      const float rq = rstd * (0.22360679774997896f * 1.4426950408889634f);
#pragma unroll
      for (int g = 0; g < 4; ++g) {
        const f32x4 c0 = *(LAS f32x4*)(L_M + m * 256 + g * 32 + hh * 16);
        const f32x4 c1 = *(LAS f32x4*)(L_M + m * 256 + 128 + g * 32 + hh * 16);
        const float s0 = (g < 2) ? rq : ((g == 2 && !hh) ? rq : rstd);
#pragma unroll
        for (int i = 0; i < 4; ++i) {
          a0[4 * g + i] = (a0[4 * g + i] - mean * c0[i]) * s0;
          a1[4 * g + i] = (a1[4 * g + i] - mean * c1[i]) * rstd;
        }
      }
      q1 = (u32x4){pack2(a0[0], a0[1]), pack2(a0[2], a0[3]), pack2(a0[4], a0[5]), pack2(a0[6], a0[7])};
      q2 = hh ? (u32x2){0u, 0u} : (u32x2){pack2(a0[8], a0[9]), pack2(a0[10], a0[11])};
      const u32x4 k1 = {pack2(a1[0], a1[1]), pack2(a1[2], a1[3]), pack2(a1[4], a1[5]), pack2(a1[6], a1[7])};
      const u32x2 k2 = hh ? (u32x2){0x3F803F80u, 0x00003F80u} : (u32x2){pack2(a1[8], a1[9]), pack2(a1[10], a1[11])};
      const unsigned kvoff = OFF_KV + layer * KV_LAYER + (h * 8 + c) * KV_CHUNK;
      lds_w128(L_K + KV_K + tt * 1536 + lane * 16, k1);
      lds_w64(L_K + KV_K + tt * 1536 + 1024 + lane * 8, k2);
      st128(k1, rs, lane * 16, kvoff + tt * 1536);
      st64(k2, rs, lane * 8, kvoff + tt * 1536 + 1024);
      const unsigned vb = L_V + KV_V + (tt * 4 + (t >> 3)) * 336 + (t & 7) * 2;
#pragma unroll
      for (int r = 0; r < 4; ++r) {
        lds_w16(vb + (4 + 4 * hh + r) * 16, a0[12 + r]);
        lds_w16(vb + (hh ? r : 16 + r) * 16, hh ? a0[8 + r] : a1[12 + r]);
        lds_w16(vb + (hh ? 12 + r : 20) * 16, hh ? a1[8 + r] : 1.0f);
      }
      const u32x4 v0 = lds_r128(L_V + KV_V + tt * 1344 + lane * 16);
      st128(v0, rs, lane * 16, kvoff + KV_K + tt * 1344);
      if (lane < 20) {
        const u32x4 v1 = lds_r128(L_V + KV_V + tt * 1344 + 1024 + lane * 16);
        st128(v1, rs, lane * 16, kvoff + KV_K + tt * 1344 + 1024);
      }
      STAMP(2 + layer * 10);
      drain_vm();
      bar_lds();
      if (tid == 0) __hip_atomic_store(flags + FL_KV + (layer * 32 + h) * 32 + c, 1u, RLX_AGENT);
      if (w >= 4) {
        const char* src = ws + OFF_WO + (size_t)(m * 8 + (h & 7)) * WO_CG + lane * 16;
        for (int i = w - 4; i < 100; i += 4) dma1k(L_A + i * 1024, src + i * 1024);
      }
    }
    STAMP(3 + layer * 10);

    {
      LANE_VARS;
      const unsigned kvl = OFF_KV + layer * KV_LAYER + h * 8 * KV_CHUNK;
      const int vrow = t <= 20 ? t : (t < 28 ? 20 : 4);
      const unsigned vl = hh * 336 + vrow * 16;
      f32x16 o = zero16, sa, sb = zero16;
      Frag f;
      f.vb = (u32x4){0u, 0u, 0u, 0u};
      if (m == 1) {
        unsigned kc = L_K + KV_K + lane * 16, kn = L_K + lane * 16;
        unsigned k2c = L_K + KV_K + 1024 + lane * 8, k2n = L_K + 1024 + lane * 8;
        unsigned vc = L_V + KV_V + vl, vn = L_V + 2 * KV_V + vl;
        int vi = 2;
        f.k1 = lds_r128(kc);
        f.k2 = lds_r64(k2c);
        sa = mfma32k8(f.k2, q2, mfma32(f.k1, q1, zero16));
        f.k1 = lds_r128(kc + 1536);
        f.k2 = lds_r64(k2c + 1536);
        f.va = lds_r128(vc);
        float mx = max16(sa);
        mx = fmaxf(mx, xor32(mx));
        set_shift(q2, mx, hh);
#pragma unroll
        for (int i = 0; i < 16; ++i) sa[i] -= mx;
        wait_flags(flags + FL_KV + (layer * 32 + h) * 32, 8, lane, tmo);
        STAMP(4 + layer * 10);
        const unsigned go2 = tid < 416 ? tid * 16 + 16384 : 0xE0000000u;
        const unsigned l2b = tid < 416 ? L_V + 4096 + tid * 16 : L_M + 1024 + (tid - 416) * 16;
        const unsigned l2s = tid < 416 ? KV_V : 0;
        u32x4 g0, g1, g2;
        {
          const unsigned so = kvl + ((c + 1) & 7) * KV_CHUNK;
          g0 = ld128(rs, tid * 16, so); g1 = ld128(rs, tid * 16 + 8192, so); g2 = ld128(rs, go2, so);
        }
        for (int j = 0; j < 8; ++j) {
          att_step<1, 1, 1>(sa, sb, o, f, q1, q2, zero16, vc + 672, kc + 2 * 1536, k2c + 2 * 1536, vc + 1344);
          att_step<1, 1, 1>(sb, sa, o, f, q1, q2, zero16, vc + 1 * 1344 + 672, kc + 3 * 1536, k2c + 3 * 1536, vc + 2 * 1344);
          att_step<1, 1, 1>(sa, sb, o, f, q1, q2, zero16, vc + 2 * 1344 + 672, kc + 4 * 1536, k2c + 4 * 1536, vc + 3 * 1344);
          att_step<1, 1, 1>(sb, sa, o, f, q1, q2, zero16, vc + 3 * 1344 + 672, kc + 5 * 1536, k2c + 5 * 1536, vc + 4 * 1344);
          att_step<1, 1, 1>(sa, sb, o, f, q1, q2, zero16, vc + 4 * 1344 + 672, kc + 6 * 1536, k2c + 6 * 1536, vc + 5 * 1344);
          {
            const unsigned kd = L_K + (j & 1) * KV_K, vd = L_V + vi * KV_V;
            if (!(ABL & 1)) {
            lds_w128(kd + tid * 16, g0);
            lds_w128((w < 4 ? kd + 8192 : vd - 4096) + tid * 16, g1);
            lds_w128(l2b + vi * l2s, g2);
            } else asm volatile("" :: "v"(g0), "v"(g1), "v"(g2));
            const unsigned so = kvl + ((c + j + 2) & 7) * KV_CHUNK + (((5 - j) >> 31) & 0x0F000000);
            if (!(ABL & 2)) { g0 = ld128(rs, tid * 16, so); g1 = ld128(rs, tid * 16 + 8192, so); g2 = ld128(rs, go2, so); }
            SB();
          }
          att_step<1, 1, 1>(sb, sa, o, f, q1, q2, zero16, vc + 5 * 1344 + 672, kc + 7 * 1536, k2c + 7 * 1536, vc + 6 * 1344);
          if (!(ABL & 4)) bar_lds();
          att_step<1, 1, 1>(sa, sb, o, f, q1, q2, zero16, vc + 6 * 1344 + 672, kn, k2n, vc + 7 * 1344);
          att_step<1, 1, 1>(sb, sa, o, f, q1, q2, zero16, vc + 7 * 1344 + 672, kn + 1536, k2n + 1536, vn);
          { const unsigned x = kc; kc = kn; kn = x; }
          { const unsigned x = k2c; k2c = k2n; k2n = x; }
          vc = vn;
          vi = vi == 2 ? 0 : vi + 1;
          vn = L_V + vi * KV_V + vl;
        }
        o = pv_hi(f.vb, sb, o);
      } else {
        if (w < 4) {
          u32x4 g[6];
          if (c > 0) {
            wait_flags(flags + FL_KV + (layer * 32 + h) * 32 + c - 1, 1, lane, tmo);
            const unsigned so = kvl + (c - 1) * KV_CHUNK;
#pragma unroll
            for (int i = 0; i < 5; ++i) g[i] = ld128(rs, tid * 16 + i * 4096, so);
            g[5] = ld128(rs, tid < 160 ? tid * 16 + 20480 : 0xF0000000u, so);
          } else {
#pragma unroll
            for (int i = 0; i < 6; ++i) {
              const unsigned one = (i < 3 && (tid * 16 + i * 4096) % 1536 >= 1280) ? 0x3F800000u : 0u;
              g[i] = (u32x4){0u, one, 0u, one};
            }
          }
#pragma unroll
          for (int i = 0; i < 3; ++i) lds_w128(L_K + tid * 16 + i * 4096, g[i]);
          lds_w128(L_V + tid * 16, g[3]);
          lds_w128(L_V + 4096 + tid * 16, g[4]);
          if (tid < 160) lds_w128(L_V + 8192 + tid * 16, g[5]);
          bar_lds();
        }
        const unsigned ka = L_K + tt * 1536 + lane * 16, k2a = L_K + tt * 1536 + 1024 + lane * 8, va = L_V + tt * 1344 + vl;
        f.k1 = lds_r128(ka + 8 * 1536);
        f.k2 = lds_r64(k2a + 8 * 1536);
        sa = mfma32k8(f.k2, q2, mfma32(f.k1, q1, tri_mask(t, hh, true)));
        f.k1 = lds_r128(ka + 7 * 1536);
        f.k2 = lds_r64(k2a + 7 * 1536);
        f.va = lds_r128(va + 8 * 1344);
        float mx = max16(sa);
        mx = fmaxf(mx, xor32(mx));
        set_shift(q2, mx, hh);
#pragma unroll
        for (int i = 0; i < 16; ++i) sa[i] -= mx;
        const f32x16 far = tri_mask(t, hh, false);
        att_step<1, 1, 1>(sa, sb, o, f, q1, q2, zero16, va + 8 * 1344 + 672, ka + 6 * 1536, k2a + 6 * 1536, va + 7 * 1344);
        att_step<1, 1, 1>(sb, sa, o, f, q1, q2, zero16, va + 7 * 1344 + 672, ka + 5 * 1536, k2a + 5 * 1536, va + 6 * 1344);
        att_step<1, 1, 1>(sa, sb, o, f, q1, q2, zero16, va + 6 * 1344 + 672, ka + 4 * 1536, k2a + 4 * 1536, va + 5 * 1344);
        bar_if_eq(tt, 4);
        att_step<1, 1, 1>(sb, sa, o, f, q1, q2, zero16, va + 5 * 1344 + 672, ka + 3 * 1536, k2a + 3 * 1536, va + 4 * 1344);
        bar_if_eq(tt, 5);
        att_step<1, 1, 1>(sa, sb, o, f, q1, q2, zero16, va + 4 * 1344 + 672, ka + 2 * 1536, k2a + 2 * 1536, va + 3 * 1344);
        bar_if_eq(tt, 6);
        att_step<1, 1, 1>(sb, sa, o, f, q1, q2, zero16, va + 3 * 1344 + 672, ka + 1 * 1536, k2a + 1 * 1536, va + 2 * 1344);
        bar_if_eq(tt, 7);
        att_step<1, 1, 1>(sa, sb, o, f, q1, q2, zero16, va + 2 * 1344 + 672, ka, k2a, va + 1 * 1344);
        att_step<1, 0, 1>(sb, sa, o, f, q1, q2, far, va + 1 * 1344 + 672, 0, 0, va);
        att_step<0, 0, 0>(sa, sb, o, f, q1, q2, zero16, va + 672, 0, 0, 0);
        o = pv_hi(f.vb, sa, o);
      }
      STAMP(5 + layer * 10);
      float big = fabsf(o[0]);
#pragma unroll
      for (int i = 1; i < 8; ++i) big = fmaxf(big, fabsf(o[i]));
      bool bad = !(big <= 3.0e38f);
      if (hh) bad |= !(o[8] <= SUM_LIMIT);
      else bad |= !(fabsf(o[8]) <= 3.0e38f && fabsf(o[9]) <= 3.0e38f && fabsf(o[10]) <= 3.0e38f && fabsf(o[11]) <= 3.0e38f);
#pragma unroll
      for (int i = 0; i < 8; ++i) bad |= o[i] != o[i];
      if (__builtin_amdgcn_ballot_w64(bad) != 0) {
        const int qt = c * 8 + tt;
        const int lo = m ? 0 : (qt >= 8 ? qt - 8 : 0), hi = m ? 63 : qt;
        if (hh) { q2.x = 0u; q2.y = 0u; }
        float mx = -3.0e38f;
        for (int pass = 0; pass < 2; ++pass) {
          o = zero16;
          for (int kt = lo; kt <= hi; ++kt) {
            const unsigned so = kvl + (kt >> 3) * KV_CHUNK;
            const u32x4 k1 = ld128(rs, lane * 16 + (kt & 7) * 1536, so);
            const u32x2 k2 = ld64(rs, lane * 8 + (kt & 7) * 1536 + 1024, so);
            f32x16 cin = zero16;
            if (!m && kt == qt) cin = tri_mask(t, hh, true);
            if (!m && kt == qt - 8) cin = tri_mask(t, hh, false);
            f32x16 s = mfma32k8(k2, q2, mfma32(k1, q1, cin));
            if (pass == 0) {
              mx = fmaxf(mx, max16(s));
            } else {
              const u32x4 v0 = ld128(rs, KV_K + (kt & 7) * 1344 + vl, so), v1 = ld128(rs, KV_K + (kt & 7) * 1344 + 672 + vl, so);
#pragma unroll
              for (int e = 0; e < 16; ++e) s[e] = __builtin_amdgcn_exp2f(s[e]);
              o = pv_lo(v0, s, o);
              o = pv_hi(v1, s, o);
            }
          }
          if (pass == 0) { mx = fmaxf(mx, xor32(mx)); set_shift(q2, mx, hh); }
        }
      }
      const float lx = xor32(o[8]);
      const float inv = __builtin_amdgcn_rcpf(hh ? o[8] : lx);
      const u32x4 p = {pack2(o[0] * inv, o[1] * inv), pack2(o[2] * inv, o[3] * inv), pack2(o[4] * inv, o[5] * inv), pack2(o[6] * inv, o[7] * inv)};
      const unsigned oo = OFF_O + layer * XIMG + (c * 16 + tt * 2) * 20480;
      const unsigned lo = (t >> 4) * 20480 + (t & 15) * 16;
      st128(p, rs, lo + hh * 256, oo + h * 512);
      if (!hh) st64((u32x2){pack2(o[8] * inv, o[9] * inv), pack2(o[10] * inv, o[11] * inv)}, rs, lo, oo + (64 + (h >> 1)) * 256 + (h & 1) * 8);
      drain_vm();
      bar_lds();
      if (tid == 0) __hip_atomic_store(flags + FL_O + (layer * 8 + c) * 32 + h, 1u, RLX_AGENT);
    }
    STAMP(6 + layer * 10);

    {
      LANE_VARS;
      const char* wq_next = ws + OFF_WQ + (size_t)((m ^ 1) * 32 + h) * WQ_HEAD + lane * 16;
      if (w >= 4) {
        if (layer < 3) for (int i = w - 4; i < 2 * KS_B; i += 4) dma1k(L_K + i * 1024, wq_next + i * 1024);
      } else {
        const int cg = h & 7, t16 = lane & 15, g = lane >> 4;
        const int row = c * 256 + (h >> 3) * 64 + w * 16 + t16;
        const unsigned xl = (row >> 5) * 40960 + (row & 31) * 16 + cg * 5120;
        const unsigned xo = OFF_X + layer * XIMG;
        const u32x4 ra = ld128(rs, xl + g * 512, xo), rb = ld128(rs, xl + (4 + g) * 512, xo);
        const u32x2 rc = ld64(rs, xl + (8 + (g >> 1)) * 512 + (g & 1) * 8, xo);
        wait_flags(flags + FL_O + (layer * 8 + c) * 32, 32, lane, tmo);
        STAMP(7 + layer * 10);
        const unsigned oo = OFF_O + layer * XIMG + (c * 16 + (h >> 3) * 4 + w) * 20480;
        u32x4 ob[20];
#pragma unroll
        for (int ks = 0; ks < 20; ++ks) ob[ks] = ld128(rs, lane * 16 + (ks & 3) * 1024, oo + (ks >> 2) * 4096);
        SB();
        f32x4 acc[5];
#pragma unroll
        for (int n = 0; n < 5; ++n) acc[n] = (f32x4){0.f, 0.f, 0.f, 0.f};
        const unsigned wa0 = opaque(L_A + lane * 16), wa1 = opaque(L_A + 51200 + lane * 16);
        u32x4 wf[10];
#pragma unroll
        for (int i = 0; i < 8; ++i) wf[i] = lds_r128(wa0 + i * 1024);
#pragma unroll
        for (int i = 0; i < 100; ++i) {
          const int in = i + 8;
          if (in < 100) wf[in % 10] = lds_r128(in < 50 ? wa0 + in * 1024 : wa1 + (in - 50) * 1024);
          acc[i % 5] = mfma16(wf[i % 10], ob[i / 5], acc[i % 5]);
          SB();
        }
        STAMP(8 + layer * 10);
        const u32x4 na = {pack2(bf_lo(ra.x) + acc[0][0], bf_hi(ra.x) + acc[0][1]), pack2(bf_lo(ra.y) + acc[0][2], bf_hi(ra.y) + acc[0][3]),
                          pack2(bf_lo(ra.z) + acc[1][0], bf_hi(ra.z) + acc[1][1]), pack2(bf_lo(ra.w) + acc[1][2], bf_hi(ra.w) + acc[1][3])};
        const u32x4 nb = {pack2(bf_lo(rb.x) + acc[2][0], bf_hi(rb.x) + acc[2][1]), pack2(bf_lo(rb.y) + acc[2][2], bf_hi(rb.y) + acc[2][3]),
                          pack2(bf_lo(rb.z) + acc[3][0], bf_hi(rb.z) + acc[3][1]), pack2(bf_lo(rb.w) + acc[3][2], bf_hi(rb.w) + acc[3][3])};
        const u32x2 nc = {pack2(bf_lo(rc.x) + acc[4][0], bf_hi(rc.x) + acc[4][1]), pack2(bf_lo(rc.y) + acc[4][2], bf_hi(rc.y) + acc[4][3])};
        if (layer < 3) {
          st128(na, rs, xl + g * 512, xo + XIMG);
          st128(nb, rs, xl + (4 + g) * 512, xo + XIMG);
          st64(nc, rs, xl + (8 + (g >> 1)) * 512 + (g & 1) * 8, xo + XIMG);
        } else {
          const unsigned ol = row * 1280 + cg * 160;
          __builtin_amdgcn_raw_buffer_store_b128(na, ro, ol + g * 16, 0, 0);
          __builtin_amdgcn_raw_buffer_store_b128(nb, ro, ol + 64 + g * 16, 0, 0);
          __builtin_amdgcn_raw_buffer_store_b64(nc, ro, ol + 128 + g * 8, 0, 0);
        }
      }
      if (layer < 3) {
        drain_vm();
        bar_lds();
        if (tid == 0) __hip_atomic_store(flags + FL_X + ((layer + 1) * 8 + c) * 32 + h, 1u, RLX_AGENT);
        for (int i = 2 * KS_B + w; i < 80; i += 8) dma1k(L_A + (i - 2 * KS_B) * 1024, wq_next + i * 1024);
      }
    }
    STAMP(9 + layer * 10);
  }

}

extern "C" void kernel_launch(void* const* d_in, const int* in_sizes, int n_in, void* d_out, int out_size, void* d_ws, size_t ws_size,
                              hipStream_t stream) {
  (void)in_sizes; (void)n_in; (void)out_size; (void)ws_size;
  (void)hipFuncSetAttribute((const void*)main_kernel, hipFuncAttributeMaxDynamicSharedMemorySize, LDS_BYTES);
  setup_kernel<<<PB_TOTAL, 512, 0, stream>>>((const u16*)d_in[0], (const u16*)d_in[1], (const u16*)d_in[2], (const u16*)d_in[4],
                                             (const u16*)d_in[5], (char*)d_ws);
  main_kernel<<<256, 512, LDS_BYTES, stream>>>((char*)d_ws, (u16*)d_out);
}
```

```cpp
#include <hip/hip_runtime.h>
#include <hip/hip_bf16.h>

typedef float f32x16 __attribute__((ext_vector_type(16)));
typedef float f32x8 __attribute__((ext_vector_type(8)));
typedef float f32x4 __attribute__((ext_vector_type(4)));
typedef unsigned u32x4 __attribute__((ext_vector_type(4)));
typedef unsigned u32x2 __attribute__((ext_vector_type(2)));
typedef __bf16 bf16x16 __attribute__((ext_vector_type(16)));
typedef __bf16 bf16x8 __attribute__((ext_vector_type(8)));
typedef __bf16 bf16x2 __attribute__((ext_vector_type(2)));
typedef short s16x4 __attribute__((ext_vector_type(4)));
typedef unsigned short u16;

#define LAS __attribute__((address_space(3)))
#define GAS __attribute__((address_space(1)))
typedef GAS unsigned gu32;
#define RLX_AGENT __ATOMIC_RELAXED, __HIP_MEMORY_SCOPE_AGENT
#define DEVI __device__ __forceinline__

constexpr unsigned WQ_HEAD = 81920;
constexpr unsigned WO_CG = 102400;
constexpr unsigned XIMG = 2621440;
constexpr unsigned XT = 41984, XIM2 = 64 * XT;
constexpr unsigned VOCT = 352, VT = 4 * VOCT;
constexpr unsigned KV_K = 12288, KV_V = 8 * VT, KV_CHUNK = KV_K + KV_V;
constexpr unsigned KV_LAYER = 32 * 8 * KV_CHUNK;
constexpr unsigned OFF_FLAG = 0;
constexpr unsigned FLAG_BYTES = 65536;
constexpr unsigned OFF_WQ = FLAG_BYTES;
constexpr unsigned OFF_WO = OFF_WQ + 2 * 32 * WQ_HEAD;
constexpr unsigned OFF_CS = OFF_WO + 2 * 8 * WO_CG;
constexpr unsigned OFF_X = OFF_CS + 2 * 10 * 1920 * 4;
constexpr unsigned OFF_O = OFF_X + 4 * XIM2;
constexpr unsigned OFF_KV = OFF_O + 4 * XIMG;
constexpr unsigned WS_USED = OFF_KV + 4 * KV_LAYER;
constexpr unsigned OFF_DBG = WS_USED;
constexpr unsigned FL_R = 0;
constexpr unsigned FL_TMO = 32 * 32;
constexpr unsigned SENT = 0xFFFFFFFFu;
constexpr unsigned OOB = 0xE0000000u;

constexpr unsigned L_A = 0;
constexpr unsigned L_K = 102400;
constexpr unsigned L_V = L_K + 2 * KV_K;
constexpr unsigned L_M = L_V + 3 * KV_V;
constexpr unsigned L_S = L_K + 18 * 2048;
constexpr unsigned LDS_BYTES = 163840;
constexpr int KS_B = 18;

constexpr unsigned kSpinCap = 1u << 18;
#define NODOT 0
#define ABL 0
#define XPG 2
#define OWD 10
#define HPRIO 3
#define KVDMA 1
#define PACE 3
#define XPD 16
#define MAPC 2
#define SUM_LIMIT 1.0e18f

DEVI unsigned pack2(float a, float b) {
  bf16x2 v = {(__bf16)a, (__bf16)b};
  return __builtin_bit_cast(unsigned, v);
}
DEVI float sq2(unsigned u, float acc) {
  const bf16x2 p = __builtin_bit_cast(bf16x2, u);
  return __builtin_amdgcn_fdot2_f32_bf16(p, p, acc, false);
}
DEVI float bf_lo(unsigned u) { return __uint_as_float(u << 16); }
DEVI float bf_hi(unsigned u) { return __uint_as_float(u & 0xffff0000u); }
DEVI f32x16 mfma32(u32x4 a, u32x4 b, f32x16 c) {
  return __builtin_amdgcn_mfma_f32_32x32x16_bf16(__builtin_bit_cast(bf16x8, a), __builtin_bit_cast(bf16x8, b), c, 0, 0, 0);
}
DEVI f32x16 mfma32k8(u32x2 a, u32x2 b, f32x16 c) {
  return __builtin_amdgcn_mfma_f32_32x32x8bf16_1k(__builtin_bit_cast(s16x4, a), __builtin_bit_cast(s16x4, b), c, 0, 0, 0);
}
DEVI f32x4 mfma16(u32x4 a, u32x4 b, f32x4 c) {
  return __builtin_amdgcn_mfma_f32_16x16x32_bf16(__builtin_bit_cast(bf16x8, a), __builtin_bit_cast(bf16x8, b), c, 0, 0, 0);
}
DEVI f32x16 pv_lo(u32x4 v, const f32x16& p, f32x16 o) {
  f32x8 h = __builtin_shufflevector(p, p, 0, 1, 2, 3, 4, 5, 6, 7);
  return __builtin_amdgcn_smfmac_f32_32x32x32_bf16(__builtin_bit_cast(bf16x8, v), __builtin_bit_cast(bf16x16, h), o, 0xDDDD, 0, 0);
}
DEVI f32x16 pv_hi(u32x4 v, const f32x16& p, f32x16 o) {
  f32x8 h = __builtin_shufflevector(p, p, 8, 9, 10, 11, 12, 13, 14, 15);
  return __builtin_amdgcn_smfmac_f32_32x32x32_bf16(__builtin_bit_cast(bf16x8, v), __builtin_bit_cast(bf16x16, h), o, 0xDDDD, 0, 0);
}
DEVI u32x4 lds_r128(unsigned a) { return *(LAS u32x4*)a; }
DEVI u32x2 lds_r64(unsigned a) { return *(LAS u32x2*)a; }
DEVI void lds_w128(unsigned a, u32x4 v) { *(LAS u32x4*)a = v; }
DEVI void lds_w64(unsigned a, u32x2 v) { *(LAS u32x2*)a = v; }
DEVI void lds_w16(unsigned a, float v) { *(LAS __bf16*)a = (__bf16)v; }
DEVI float half_lo(float v) { return __uint_as_float(__builtin_amdgcn_permlane32_swap(__float_as_uint(v), __float_as_uint(v), false, false)[0]); }
DEVI float half_hi(float v) { return __uint_as_float(__builtin_amdgcn_permlane32_swap(__float_as_uint(v), __float_as_uint(v), false, false)[1]); }
DEVI unsigned umax(unsigned a, unsigned b) { return a > b ? a : b; }
DEVI unsigned umax4(unsigned m, u32x4 v) { return umax(umax(umax(umax(m, v.x), v.y), v.z), v.w); }
DEVI bool unwritten(unsigned m) { return __builtin_amdgcn_ballot_w64(m == SENT) != 0; }
DEVI unsigned clean(unsigned v) { return v < SENT ? v : SENT - 1; }
DEVI u32x4 clean4(u32x4 v) { return (u32x4){clean(v.x), clean(v.y), clean(v.z), clean(v.w)}; }
DEVI u32x2 clean2(u32x2 v) { return (u32x2){clean(v.x), clean(v.y)}; }
constexpr int vpos(int d) {
  return d < 4 ? 16 + d : d < 10 ? d - 4 : d < 12 ? d - 3 : d < 14 ? d + 8 : d < 16 ? d - 1 : d < 20 ? d - 7 : d == 20 ? 15 : 6;
}
#define SB() __builtin_amdgcn_sched_barrier(0)
DEVI void bar_lds() {
  asm volatile("s_waitcnt lgkmcnt(0)\n\ts_barrier" ::: "memory");
}
DEVI void drain_vm() { asm volatile("s_waitcnt vmcnt(0)" ::: "memory"); }
#define DMA_ASM(d, v, r, o) "s_mov_b32 m0, " d "\n\ts_nop 3\n\tbuffer_load_dwordx4 " v ", " r ", " o " offen sc1 lds\n\t"
DEVI void dma1k(unsigned lds_dst, unsigned voff, u32x4 srd, unsigned soff) {
  unsigned keep;
  asm volatile("s_mov_b32 %0, m0\n\t" DMA_ASM("%1", "%2", "%3", "%4") "s_mov_b32 m0, %0"
               : "=&s"(keep) : "s"(lds_dst), "v"(voff), "s"(srd), "s"(soff) : "memory");
}
constexpr unsigned NO_LDS = 0x30000;
typedef __amdgpu_buffer_rsrc_t rsrc_t;
DEVI u32x4 ld128(rsrc_t r, unsigned voff, unsigned soff) { return __builtin_amdgcn_raw_buffer_load_b128(r, voff, soff, 16); }
DEVI u32x2 ld64(rsrc_t r, unsigned voff, unsigned soff) { return __builtin_amdgcn_raw_buffer_load_b64(r, voff, soff, 16); }
template <int AUX> DEVI void st128a(u32x4 v, rsrc_t r, unsigned voff, unsigned soff) {
  __builtin_amdgcn_raw_buffer_store_b128(v, r, voff, soff, AUX);
  asm volatile("s_nop 1" :: "v"(v) : "memory");
}
DEVI void st128(u32x4 v, rsrc_t r, unsigned voff, unsigned soff) { st128a<16>(v, r, voff, soff); }
DEVI void st64(u32x2 v, rsrc_t r, unsigned voff, unsigned soff) { __builtin_amdgcn_raw_buffer_store_b64(v, r, voff, soff, 16); }

DEVI bool give_up(unsigned& spins, gu32* tmo) {
  __builtin_amdgcn_s_sleep(2);
  if (++spins > kSpinCap) { __hip_atomic_store(tmo, 1u, RLX_AGENT); return true; }
  return (spins & 255u) == 0u && __builtin_amdgcn_readfirstlane(__hip_atomic_load(tmo, RLX_AGENT));
}
DEVI void wait_flags(gu32* words, int n, int lane, gu32* tmo) {
  for (unsigned spins = 0;;) {
    unsigned v = 1;
    if (lane < n) v = __hip_atomic_load(words + lane, RLX_AGENT);
    if (__builtin_amdgcn_ballot_w64(v == 0) == 0) break;
    __builtin_amdgcn_s_sleep(1);
    if (++spins > kSpinCap) { if (lane == 0) __hip_atomic_store(tmo, 1u, RLX_AGENT); break; }
    if ((spins & 255u) == 0u && __builtin_amdgcn_readfirstlane(__hip_atomic_load(tmo, RLX_AGENT))) break;
  }
  __builtin_amdgcn_fence(__ATOMIC_ACQUIRE, "wavefront");
}

#define STAMP(i) do {} while (0)

constexpr int PB_WQ = 2 * 10 * 8;
constexpr int PB_WO = 2 * 10 * 3;
constexpr int PB_X = 320;
constexpr int PB_R = 256;
constexpr int PB_TOTAL = PB_WQ + PB_WO + PB_X + PB_R + 1;

DEVI void transpose8x4(const u32x2 (&r)[8], u32x4 (&o)[4]) {
#pragma unroll
  for (int p = 0; p < 4; ++p) {
    o[0][p] = __builtin_amdgcn_perm(r[2 * p + 1].x, r[2 * p].x, 0x05040100u);
    o[1][p] = __builtin_amdgcn_perm(r[2 * p + 1].x, r[2 * p].x, 0x07060302u);
    o[2][p] = __builtin_amdgcn_perm(r[2 * p + 1].y, r[2 * p].y, 0x05040100u);
    o[3][p] = __builtin_amdgcn_perm(r[2 * p + 1].y, r[2 * p].y, 0x07060302u);
  }
}

__global__ void __launch_bounds__(512) setup_kernel(const u16* __restrict__ x, const u16* __restrict__ wqA, const u16* __restrict__ woA,
                                                    const u16* __restrict__ wqB, const u16* __restrict__ woB, char* __restrict__ ws) {
  __shared__ float red[8][256];
  const int tid = threadIdx.x, lane = tid & 63, w = tid >> 6;
  int b = blockIdx.x;
  if (b < PB_WQ) {
    const int m = b / 80, kb = (b % 80) / 8, cq = (b % 8) * 64 + lane;
    const int kh = kb * 8 + w;
    const u16* W = m ? wqB : wqA;
    float s[4] = {0.f, 0.f, 0.f, 0.f};
    if (cq < 480) {
      u32x2 r[8];
#pragma unroll
      for (int j = 0; j < 8; ++j) r[j] = *(const u32x2*)(W + (size_t)(kh * 8 + j) * 1920 + cq * 4);
      u32x4 o[4];
      transpose8x4(r, o);
#pragma unroll
      for (int j = 0; j < 8; ++j) { s[0] += bf_lo(r[j].x); s[1] += bf_hi(r[j].x); s[2] += bf_lo(r[j].y); s[3] += bf_hi(r[j].y); }
      const int which = cq / 160, h = (cq % 160) / 5, d = (cq % 5) * 4;
      int mt, row;
      if (which == 0) { mt = 0; row = d; }
      else if (which == 1) { mt = 1; row = d; }
      else if (d < 12) { mt = 0; row = 20 + d; }
      else { mt = 1; row = 8 + d; }
      char* dst = ws + OFF_WQ + (size_t)(m * 32 + h) * WQ_HEAD + (kh >> 1) * 2048 + (kh & 1) * 512;
      u32x4* p = (u32x4*)(dst + mt * 1024 + row * 16);
#pragma unroll
      for (int i = 0; i < 4; ++i) p[i] = o[i];
      if (which == 2 && d == 16) {
        const u32x4 one = {0x3F803F80u, 0x3F803F80u, 0x3F803F80u, 0x3F803F80u}, zero = {0u, 0u, 0u, 0u};
        p[4] = one; p[5] = zero; p[6] = zero; p[7] = zero;
      }
    }
#pragma unroll
    for (int i = 0; i < 4; ++i) red[w][lane * 4 + i] = s[i];
    __syncthreads();
    if (tid < 256) {
      float t = 0.f;
#pragma unroll
      for (int i = 0; i < 8; ++i) t += red[i][tid];
      const int col = (b % 8) * 256 + tid;
      if (col < 1920) ((float*)(ws + OFF_CS))[(m * 10 + kb) * 1920 + col] = t;
    }
    return;
  }
  b -= PB_WQ;
  if (b < PB_WO) {
    const int m = b / 30, q = ((b % 30) / 3) * 8 + w, cq = (b % 3) * 64 + lane;
    if (cq >= 160) return;
    const u16* W = m ? woB : woA;
    int k0, k1;
    if (q < 64) { k0 = (q >> 1) * 20 + (q & 1) * 4; k1 = k0 + 8; }
    else { k0 = (q - 64) * 40 + 16; k1 = k0 + 20; }
    u32x2 r[8];
#pragma unroll
    for (int j = 0; j < 4; ++j) {
      r[j] = *(const u32x2*)(W + (size_t)(k0 + j) * 640 + cq * 4);
      r[4 + j] = *(const u32x2*)(W + (size_t)(k1 + j) * 640 + cq * 4);
    }
    u32x4 o[4];
    transpose8x4(r, o);
    const int cg = cq / 20, c4 = cq % 20;
    int nt, g;
    if (c4 < 16) { nt = (c4 >> 3) * 2 + (c4 & 1); g = (c4 >> 1) & 3; }
    else { nt = 4; g = c4 - 16; }
    char* dst = ws + OFF_WO + (size_t)(m * 8 + cg) * WO_CG + ((q >> 2) * 5 + nt) * 1024 + (q & 3) * 256 + g * 64;
#pragma unroll
    for (int i = 0; i < 4; ++i) ((u32x4*)dst)[i] = o[i];
    return;
  }
  b -= PB_WO;
  if (b < PB_X) {
    const int unit = b * 8 + w;
    const int row = (unit / 10) * 8 + (lane & 7), ch = (unit % 10) * 8 + (lane >> 3);
    const u32x4 v = clean4(*(const u32x4*)(x + (size_t)row * 640 + ch * 8));
    *(u32x4*)(ws + OFF_X + (row >> 5) * XT + ch * 512 + (row & 31) * 16) = v;
    return;
  }
  b -= PB_X;
  if (b < PB_R) {
    const u32x4 sv = {SENT, SENT, SENT, SENT};
    for (unsigned i = b * 512 + tid; i < 2 * KV_LAYER / 16; i += PB_R * 512) ((u32x4*)(ws + OFF_KV))[i] = sv;
    for (unsigned i = b * 512 + tid; i < 2 * XIMG / 16; i += PB_R * 512) ((u32x4*)(ws + OFF_O))[i] = sv;
    for (unsigned i = b * 512 + tid; i < XIM2 / 16; i += PB_R * 512) ((u32x4*)(ws + OFF_X + XIM2))[i] = sv;
    if (b < 32) {
      const int row = b * 64 + (tid >> 3), p = tid & 7;
      float ss = 0.f;
#pragma unroll
      for (int i = 0; i < 10; ++i) { const u32x4 v = *(const u32x4*)(x + (size_t)row * 640 + (p + 8 * i) * 8); ss = sq2(v.x, ss); ss = sq2(v.y, ss); ss = sq2(v.z, ss); ss = sq2(v.w, ss); }
      *(unsigned*)(ws + OFF_X + (row >> 5) * XT + 40960 + (row & 31) * 4 + p * 128) = clean(__float_as_uint(ss));
    }
    return;
  }
  const u32x4 zero = {0u, 0u, 0u, 0u};
  for (int i = tid; i < (int)(FLAG_BYTES / 16); i += 512) ((u32x4*)(ws + OFF_FLAG))[i] = zero;
}

struct Frag { u32x4 k1; u32x2 k2; u32x4 va, vb; };

template <bool QK, bool LDK, bool LDV>
DEVI void att_step(f32x16& cur, f32x16& oth, f32x16& o, Frag& f, const u32x4& q1, const u32x2& q2, const f32x16& cin,
                   unsigned a_vb, unsigned a_k1, unsigned a_k2, unsigned a_va) {
  o = pv_hi(f.vb, oth, o);
  f.vb = lds_r128(a_vb);
  SB();
#pragma unroll
  for (int i = 0; i < 4; ++i) cur[i] = __builtin_amdgcn_exp2f(cur[i]);
  SB();
  if (QK) oth = mfma32(f.k1, q1, cin);
  if (LDK) f.k1 = lds_r128(a_k1);
  SB();
#pragma unroll
  for (int i = 4; i < 8; ++i) cur[i] = __builtin_amdgcn_exp2f(cur[i]);
  SB();
  if (QK) oth = mfma32k8(f.k2, q2, oth);
  if (LDK) f.k2 = lds_r64(a_k2);
  SB();
#pragma unroll
  for (int i = 8; i < 12; ++i) cur[i] = __builtin_amdgcn_exp2f(cur[i]);
  SB();
  o = pv_lo(f.va, cur, o);
  if (LDV) f.va = lds_r128(a_va);
  SB();
#pragma unroll
  for (int i = 12; i < 16; ++i) cur[i] = __builtin_amdgcn_exp2f(cur[i]);
  SB();
}
DEVI void bar_if_eq(int a, int b) {
  asm volatile("s_cmp_lg_u32 %0, %1\n\ts_cbranch_scc1 .Lskip%=\n\ts_waitcnt lgkmcnt(0)\n\ts_barrier\n.Lskip%=:" :: "s"(a), "s"(b) : "memory", "scc");
}
DEVI void bar_if_ne(int a, int b) {
  asm volatile("s_cmp_eq_u32 %0, %1\n\ts_cbranch_scc1 .Lskip%=\n\ts_waitcnt lgkmcnt(0)\n\ts_barrier\n.Lskip%=:" :: "s"(a), "s"(b) : "memory", "scc");
}
DEVI int opaque(int v) { asm volatile("" : "+v"(v)); return v; }

DEVI float max16(const f32x16& s) {
  float m = fmaxf(fmaxf(s[0], s[1]), s[2]);
#pragma unroll
  for (int i = 3; i < 15; i += 2) m = fmaxf(fmaxf(m, s[i]), s[i + 1]);
  return fmaxf(m, s[15]);
}
DEVI void set_shift(u32x2& q2, float m, int hh) {
  const unsigned hi = __float_as_uint(m) & 0xffff0000u;
  const float r1 = m - __uint_as_float(hi);
  const unsigned mid = __float_as_uint(r1) & 0xffff0000u;
  const float r2 = r1 - __uint_as_float(mid);
  const unsigned lo = __float_as_uint(r2) & 0xffff0000u;
  if (hh) { q2.x = ((hi >> 16) | mid) ^ 0x80008000u; q2.y = ((lo >> 16) ^ 0x8000u) | 0xFF7F0000u; }
}
DEVI f32x16 tri_mask(int t, int hh, bool diag) {
  f32x16 c;
#pragma unroll
  for (int i = 0; i < 16; ++i) {
    const int key = (i & 3) + 8 * (i >> 2) + 4 * hh;
    c[i] = ((key <= t) == diag) ? 0.f : -3.0e38f;
  }
  return c;
}

struct KvRegs { u32x4 g[6]; };
DEVI void kv_load(KvRegs& r, int u, unsigned l16, rsrc_t rs, unsigned so) {
#pragma unroll
  for (int i = 0; i < 6; ++i) r.g[i] = ld128(rs, (i == 5 && u == 3) ? OOB : l16, so + (u + 4 * i) * 1024);
}
DEVI bool kv_unwritten(const KvRegs& r) {
  unsigned mx = 0;
#pragma unroll
  for (int i = 0; i < 6; ++i) mx = umax4(mx, r.g[i]);
  return unwritten(mx);
}
DEVI void kv_put(KvRegs& r, int u, unsigned l16, rsrc_t rs, unsigned so, unsigned kd, unsigned vd, gu32* tmo, bool pace) {
  unsigned spins = 0;
  while (kv_unwritten(r) && !give_up(spins, tmo)) kv_load(r, u, l16, rs, so);
#pragma unroll
  for (int i = 0; i < 5; ++i) { lds_w128((i < 3 ? kd : vd - KV_K) + (u + 4 * i) * 1024 + l16, r.g[i]); if (pace) __builtin_amdgcn_s_sleep(PACE); }
  if (u < 3) lds_w128(vd - KV_K + (u + 20) * 1024 + l16, r.g[5]);
}

DEVI void stage_kv(int u, unsigned l16, u32x4 srd, unsigned so, unsigned kd, unsigned vd, gu32* tmo) {
  unsigned spins = 0, mx;
  do {
#pragma unroll
    for (int i = 0; i < 6; ++i) {
      const unsigned p = (i == 5 && u == 3) ? 22 : u + 4 * i;
      dma1k((i < 3 ? kd : vd - KV_K) + p * 1024, l16, srd, so + p * 1024);
    }
    drain_vm();
    mx = 0;
#pragma unroll
    for (int i = 0; i < 6; ++i) {
      const unsigned p = (i == 5 && u == 3) ? 22 : u + 4 * i;
      mx = umax4(mx, lds_r128((i < 3 ? kd : vd - KV_K) + p * 1024 + l16));
    }
  } while (unwritten(mx) && !give_up(spins, tmo));
}

__global__ void __launch_bounds__(768, 3) main_kernel(char* __restrict__ ws, u16* __restrict__ out) {
  const int tid0 = threadIdx.x;
  const int w = __builtin_amdgcn_readfirstlane(tid0 >> 6);
  const int tt = w < 4 ? w : 11 - w;
  const int xg = blockIdx.x & 7, xi = blockIdx.x >> 3;
  const int c = ((xg << MAPC) & 7) | (xi & ((1 << MAPC) - 1));
  const int h = ((xg >> (3 - MAPC)) << (5 - MAPC)) | (xi >> MAPC);
#define LANE_VARS const int tid = opaque(tid0), lane = tid & 63, t = lane & 31, hh = lane >> 5; (void)t; (void)hh
  gu32* flags = (gu32*)(ws + OFF_FLAG);
  gu32* tmo = flags + FL_TMO;
  const rsrc_t rs = __builtin_amdgcn_make_buffer_rsrc(ws, 0, WS_USED, 0x00020000);
  const rsrc_t ro = __builtin_amdgcn_make_buffer_rsrc(out, 0, XIMG, 0x00020000);
  const u32x4 srd = {(unsigned)(size_t)ws, (unsigned)((size_t)ws >> 32) & 0xffffu, WS_USED, 0x00020000u};
  const f32x16 zero16 = {0.f, 0.f, 0.f, 0.f, 0.f, 0.f, 0.f, 0.f, 0.f, 0.f, 0.f, 0.f, 0.f, 0.f, 0.f, 0.f};
  STAMP(0);

  {
    LANE_VARS;
    for (int i = w; i < 80; i += 12) dma1k(i < 2 * KS_B ? L_K + i * 1024 : L_A + (i - 2 * KS_B) * 1024, lane * 16, srd, OFF_WQ + h * WQ_HEAD + i * 1024);
    if (tid < 128) {
      const int m = tid >> 6, mt = (tid >> 5) & 1, r = tid & 31;
      int col = -1;
      if (r < 20) col = mt * 640 + h * 20 + r;
      else if (mt == 0) col = 1280 + h * 20 + r - 20;
      else if (r < 28) col = 1280 + h * 20 + r - 8;
      float s = 0.f;
      if (col >= 0) {
#pragma unroll
        for (int i = 0; i < 10; ++i) s += ((const float*)(ws + OFF_CS))[(m * 10 + i) * 1920 + col];
      }
      *(LAS float*)(L_M + tid * 4) = s;
    }
    drain_vm();
    bar_lds();
  }

  if (w >= 8) {
    const int u = w - 8, lane = tid0 & 63;
    const unsigned l16 = lane * 16;
    __builtin_amdgcn_s_setprio(HPRIO);
    for (int layer = 0; layer < 4; ++layer) {
      const int m = layer & 1;
      bar_lds();
      bar_lds();
      STAMP(1 + layer * 10);
      const unsigned kvl = OFF_KV + layer * KV_LAYER + h * 8 * KV_CHUNK;
      {
        u32x4 g[6];
#pragma unroll
        for (int i = 0; i < 6; ++i) g[i] = lds_r128((i < 3 ? L_K + KV_K : L_V + KV_V - KV_K) + (u + 4 * i) * 1024 + l16);
#pragma unroll
        for (int i = 0; i < 6; ++i) st128(clean4(g[i]), rs, (i == 5 && u == 3) ? OOB : l16, kvl + c * KV_CHUNK + (u + 4 * i) * 1024);
      }
      STAMP(2 + layer * 10);
      const unsigned wo_src = OFF_WO + (m * 8 + (h & 7)) * WO_CG + u * 1024;
      for (int i = 0; i < 12; ++i) dma1k(L_A + u * 1024 + i * 4096, l16, srd, wo_src + i * 4096);
      asm volatile("s_waitcnt vmcnt(12)" ::: "memory");
      STAMP(3 + layer * 10);
      if (m == 0) {
        if (c > 0) {
          KvRegs r;
          kv_load(r, u, l16, rs, kvl + (c - 1) * KV_CHUNK);
          kv_put(r, u, l16, rs, kvl + (c - 1) * KV_CHUNK, L_K, L_V, tmo, false);
        } else {
#pragma unroll
          for (int i = 0; i < 6; ++i) {
            const unsigned p = (i == 5 && u == 3) ? 22 : u + 4 * i;
            const unsigned one = (i < 3 && (p * 1024 + l16) % 1536 >= 1280) ? 0x3F800000u : 0u;
            lds_w128((i < 3 ? L_K : L_V - KV_K) + p * 1024 + l16, (u32x4){0u, one, 0u, one});
          }
        }
        STAMP(4 + layer * 10);
        bar_lds();
        STAMP(5 + layer * 10);
      } else {
        int vi = 2;
        KvRegs r;
        kv_load(r, u, l16, rs, kvl + ((c - 1) & 7) * KV_CHUNK);
        for (int j = 1; j < 8; ++j) {
          const unsigned so = kvl + ((c - j) & 7) * KV_CHUNK, kd = L_K + ((j + 1) & 1) * KV_K, vd = L_V + vi * KV_V;
          if (KVDMA && j > 1) {
            if (KVDMA == 1 || j == 2) stage_kv(u, l16, srd, so, kd, vd, tmo);
            else if (KVDMA == 3) {
#pragma unroll
              for (int i = 0; i < 6; ++i) {
                const unsigned p = (i == 5 && u == 3) ? 22 : u + 4 * i;
                dma1k((i < 3 ? kd : vd - KV_K) + p * 1024, l16, srd, so + p * 1024);
                if (PACE) __builtin_amdgcn_s_sleep(PACE);
              }
              drain_vm();
            }
          } else {
            kv_put(r, u, l16, rs, so, kd, vd, tmo, j > 1);
            if (!KVDMA && j < 7) kv_load(r, u, l16, rs, kvl + ((c - j - 1) & 7) * KV_CHUNK);
          }
          if (j == 1) STAMP(4 + layer * 10);
          if (j == 7) drain_vm();
          vi = vi == 2 ? 0 : vi + 1;
          if (layer == 1 && j < 7) {
            const u32x4 sv = {SENT, SENT, SENT, SENT};
            for (unsigned q = (j - 1) * 16 + u; q < (unsigned)j * 16 && q < 88; q += 4) {
              unsigned a;
              if (q < 46) a = OFF_KV + 2 * KV_LAYER + (h * 8 + c) * KV_CHUNK + (q < 23 ? q * 1024 : KV_LAYER + (q - 23) * 1024);
              else if (q < 68) { const unsigned k = q < 57 ? q - 46 : q - 57; a = OFF_X + (q < 57 ? 2 : 3) * XIM2 + c * 8 * XT + h * (XT / 4) + (k < 10 ? k * 1024 : XT / 4 - 1024); }
              else a = OFF_O + (q < 78 ? 2 * XIMG + (q - 68) * 1024 : 3 * XIMG + (q - 78) * 1024) + c * 327680 + h * 10240;
              st128(sv, rs, l16, a);
            }
          }
          bar_lds();
        }
        if (layer == 1 && tid0 == 512) __hip_atomic_store(flags + FL_R + h * 32 + c, 1u, RLX_AGENT);
        STAMP(5 + layer * 10);
      }
      for (int i = 12; i < 25; ++i) dma1k(L_A + u * 1024 + i * 4096, l16, srd, wo_src + i * 4096);
      STAMP(6 + layer * 10);
      u32x4 wn[20];
      const unsigned wq_next = layer < 3 ? OFF_WQ + ((m ^ 1) * 32 + h) * WQ_HEAD + u * 1024 : OOB;
#pragma unroll
      for (int i = 0; i < 20; ++i) wn[i] = ld128(rs, l16, wq_next + i * 4096);
      SB();
      asm volatile("s_waitcnt vmcnt(20)" ::: "memory");
      SB();
      STAMP(7 + layer * 10);
      bar_lds();
      STAMP(8 + layer * 10);
#pragma unroll
      for (int i = 0; i < KS_B / 2; ++i) lds_w128(L_K + u * 1024 + i * 4096 + l16, wn[i]);
      if (layer == 1) wait_flags(flags + FL_R + h * 32, 8, lane, tmo);
      STAMP(9 + layer * 10);
      bar_lds();
      bar_lds();
#pragma unroll
      for (int i = KS_B / 2; i < 20; ++i) lds_w128(L_A + u * 1024 + (i - KS_B / 2) * 4096 + l16, wn[i]);
    }
    return;
  }

  for (int layer = 0; layer < 4; ++layer) {
    const int m = layer & 1;
    u32x4 q1; u32x2 q2;
    {
      LANE_VARS;
      STAMP(1 + layer * 10);
      const unsigned xoff = OFF_X + layer * XIM2 + (c * 8 + tt) * XT;
      const unsigned xl = lane * 16;
#define XLD(k) ld128(rs, xl + ((k) & 3) * 1024, xoff + ((k) >> 2) * 4096)
      constexpr int PG = XPG;
      u32x4 xb[40];
      float sq[4];
#pragma unroll
      for (int ks = 0; ks < 5 * PG; ++ks) xb[ks] = XLD(ks);
      SB();
      const unsigned wb = opaque(L_K + lane * 16), wa = opaque(L_A + lane * 16);
      u32x4 wf[3][2];
#pragma unroll
      for (int ks = 0; ks < 2; ++ks) { wf[ks][0] = lds_r128(wb + ks * 2048); wf[ks][1] = lds_r128(wb + ks * 2048 + 1024); }
      f32x16 a0 = zero16, a1 = zero16;
#define SQLD(i) __uint_as_float(__builtin_amdgcn_raw_buffer_load_b32(rs, t * 4 + hh * 512 + (i) * 128, xoff + 40960, 16))
#pragma unroll
      for (int g = 0; g < 8; ++g) {
        unsigned mx = 0;
#pragma unroll
        for (int i = 0; i < 5; ++i) mx = umax4(mx, xb[5 * g + i]);
        if (__builtin_expect(unwritten(mx), 0)) {
          unsigned spins = 0;
          do {
            if (give_up(spins, tmo)) break;
#pragma unroll
            for (int i = 0; i < 5; ++i) xb[5 * g + i] = XLD(5 * g + i);
            mx = 0;
#pragma unroll
            for (int i = 0; i < 5; ++i) mx = umax4(mx, xb[5 * g + i]);
          } while (unwritten(mx));
#pragma unroll
          for (int i = 5; i < 5 * PG; ++i) if (5 * g + i < 40) xb[5 * g + i] = XLD(5 * g + i);
        }
        if (g == 0) STAMP(10 + layer * 10);
#pragma unroll
        for (int i = 0; i < 5; ++i) {
          const int ks = 5 * g + i, kn = ks + 2, kx = ks + 5 * PG;
          if (kn == KS_B) bar_lds();
          if (kn < 40) {
            const unsigned ad = kn < KS_B ? wb + kn * 2048 : wa + (kn - KS_B) * 2048;
            wf[kn % 3][0] = lds_r128(ad); wf[kn % 3][1] = lds_r128(ad + 1024);
          }
          SB();
          if (!(ABL & 8) || ks == 0) a0 = mfma32(wf[ks % 3][0], xb[ks], a0); else asm volatile("" :: "v"(wf[ks % 3][0]), "v"(xb[ks]));
          SB();
          if (!(ABL & 8) || ks == 0) a1 = mfma32(wf[ks % 3][1], xb[ks], a1); else asm volatile("" :: "v"(wf[ks % 3][1]));
          if (kx < 40) { if (ABL & 16) { xb[kx] = xb[ks]; asm volatile("" : "+v"(xb[kx])); } else xb[kx] = XLD(kx); }
          else if (kx < 44) sq[kx - 40] = SQLD(kx - 40);
          SB();
        }
      }
      f32x4 c0[4], c1[4];
#pragma unroll
      for (int g = 0; g < 4; ++g) {
        c0[g] = *(LAS f32x4*)(L_M + m * 256 + g * 32 + hh * 16);
        c1[g] = *(LAS f32x4*)(L_M + m * 256 + 128 + g * 32 + hh * 16);
      }
      STAMP(2 + layer * 10);
#define SQMAX umax(umax(__float_as_uint(sq[0]), __float_as_uint(sq[1])), umax(__float_as_uint(sq[2]), __float_as_uint(sq[3])))
      if (__builtin_expect(unwritten(SQMAX), 0)) {
        unsigned spins = 0;
        do {
          if (give_up(spins, tmo)) break;
#pragma unroll
          for (int i = 0; i < 4; ++i) sq[i] = SQLD(i);
        } while (unwritten(SQMAX));
      }
      float ss = (sq[0] + sq[1]) + (sq[2] + sq[3]);
      ss = half_lo(ss) + half_hi(ss);
      const float sum = half_hi(a1[12]);
      const float mean = sum * (1.f / 640.f);
      const float var = fmaxf(ss * (1.f / 640.f) - mean * mean, 0.f);
      const float rstd = __builtin_amdgcn_rsqf(var + 1e-5f);
      const float rq = rstd * (0.22360679774997896f * 1.4426950408889634f);
#pragma unroll
      for (int g = 0; g < 4; ++g) {
        const float s0 = (g < 2) ? rq : ((g == 2 && !hh) ? rq : rstd);
#pragma unroll
        for (int i = 0; i < 4; ++i) {
          a0[4 * g + i] = (a0[4 * g + i] - mean * c0[g][i]) * s0;
          a1[4 * g + i] = (a1[4 * g + i] - mean * c1[g][i]) * rstd;
        }
      }
      const unsigned vb = L_V + KV_V + (tt * 4 + (t >> 3)) * VOCT + (t & 7) * 2;
#pragma unroll
      for (int r = 0; r < 4; ++r) {
        lds_w16(vb + (hh ? vpos(8 + r) : vpos(4 + r)) * 16, a0[12 + r]);
        lds_w16(vb + (hh ? vpos(r) : vpos(16 + r)) * 16, hh ? a0[8 + r] : a1[12 + r]);
        lds_w16(vb + (hh ? vpos(12 + r) : vpos(20 + (r & 1))) * 16, hh ? a1[8 + r] : ((r & 1) ? 0.f : 1.0f));
      }
      lds_w128(L_K + KV_K + tt * 1536 + lane * 16, (u32x4){pack2(a1[0], a1[1]), pack2(a1[2], a1[3]), pack2(a1[4], a1[5]), pack2(a1[6], a1[7])});
      lds_w64(L_K + KV_K + tt * 1536 + 1024 + lane * 8, hh ? (u32x2){0x3F803F80u, 0x00003F80u} : (u32x2){pack2(a1[8], a1[9]), pack2(a1[10], a1[11])});
      q1 = (u32x4){pack2(a0[0], a0[1]), pack2(a0[2], a0[3]), pack2(a0[4], a0[5]), pack2(a0[6], a0[7])};
      q2 = hh ? (u32x2){0u, 0u} : (u32x2){pack2(a0[8], a0[9]), pack2(a0[10], a0[11])};
      bar_lds();
    }
    STAMP(3 + layer * 10);

    {
      LANE_VARS;
      const unsigned kvl = OFF_KV + layer * KV_LAYER + h * 8 * KV_CHUNK;
      const unsigned vl = hh * VOCT + vpos(t) * 16;
      f32x16 o = zero16, sa, sb = zero16;
      Frag f;
      f.vb = (u32x4){0u, 0u, 0u, 0u};
      if (m == 1) {
        unsigned kc = L_K + KV_K + lane * 16, kn = L_K + lane * 16;
        unsigned k2c = L_K + KV_K + 1024 + lane * 8, k2n = L_K + 1024 + lane * 8;
        unsigned vc = L_V + KV_V + vl, vn = L_V + 2 * KV_V + vl;
        f.k1 = lds_r128(kc);
        f.k2 = lds_r64(k2c);
        sa = mfma32k8(f.k2, q2, mfma32(f.k1, q1, zero16));
        f.k1 = lds_r128(kc + 1536);
        f.k2 = lds_r64(k2c + 1536);
        f.va = lds_r128(vc);
        float mx = max16(sa);
        mx = fmaxf(half_lo(mx), half_hi(mx));
        set_shift(q2, mx, hh);
#pragma unroll
        for (int i = 0; i < 16; ++i) sa[i] -= mx;
#define STEP_C(n, A, B) att_step<1, 1, 1>(A, B, o, f, q1, q2, zero16, vc + (n) * VT + 2 * VOCT, kc + ((n) + 2) * 1536, k2c + ((n) + 2) * 1536, vc + ((n) + 1) * VT)
        int vi = 0;
#define LST(j)
        for (int j = 0; j < 8; ++j) {
          LST(j);
          STEP_C(0, sa, sb);
          STEP_C(1, sb, sa);
          STEP_C(2, sa, sb);
          STEP_C(3, sb, sa);
          STEP_C(4, sa, sb);
          STEP_C(5, sb, sa);
          LST(16 + j);
          if (!(ABL & 4)) bar_if_ne(j, 7);
          LST(32 + j);
          att_step<1, 1, 1>(sa, sb, o, f, q1, q2, zero16, vc + 6 * VT + 2 * VOCT, kn, k2n, vc + 7 * VT);
          att_step<1, 1, 1>(sb, sa, o, f, q1, q2, zero16, vc + 7 * VT + 2 * VOCT, kn + 1536, k2n + 1536, vn);
          { const unsigned x = kc; kc = kn; kn = x; }
          { const unsigned x = k2c; k2c = k2n; k2n = x; }
          vc = vn;
          vn = L_V + vi * KV_V + vl;
          vi = vi == 2 ? 0 : vi + 1;
        }
        LST(8);
        o = pv_hi(f.vb, sb, o);
      } else {
        const unsigned ka = L_K + tt * 1536 + lane * 16, k2a = L_K + tt * 1536 + 1024 + lane * 8, va = L_V + tt * VT + vl;
        f.k1 = lds_r128(ka + 8 * 1536);
        f.k2 = lds_r64(k2a + 8 * 1536);
        f.va = lds_r128(va + 8 * VT);
        sa = mfma32k8(f.k2, q2, mfma32(f.k1, q1, tri_mask(t, hh, true)));
        float mx = max16(sa);
        mx = fmaxf(half_lo(mx), half_hi(mx));
        set_shift(q2, mx, hh);
#pragma unroll
        for (int i = 0; i < 16; ++i) sa[i] -= mx;
        const f32x16 far = tri_mask(t, hh, false);
        SB();
        bar_if_eq(tt, 0);
        f.k1 = lds_r128(ka + 7 * 1536);
        f.k2 = lds_r64(k2a + 7 * 1536);
#define STEP_W(n, A, B) bar_if_eq(tt, (n) + 1); \
        att_step<1, 1, 1>(A, B, o, f, q1, q2, zero16, va + (8 - (n)) * VT + 2 * VOCT, ka + (6 - (n)) * 1536, k2a + (6 - (n)) * 1536, va + (7 - (n)) * VT)
        STEP_W(0, sa, sb);
        STEP_W(1, sb, sa);
        STEP_W(2, sa, sb);
        STEP_W(3, sb, sa);
        STEP_W(4, sa, sb);
        STEP_W(5, sb, sa);
        STEP_W(6, sa, sb);
        att_step<1, 0, 1>(sb, sa, o, f, q1, q2, far, va + 1 * VT + 2 * VOCT, 0, 0, va);
        att_step<0, 0, 0>(sa, sb, o, f, q1, q2, zero16, va + 2 * VOCT, 0, 0, 0);
        o = pv_hi(f.vb, sa, o);
      }
      STAMP(5 + layer * 10);
      float big = fabsf(o[0]);
#pragma unroll
      for (int i = 1; i < 12; ++i) big = fmaxf(big, fabsf(o[i]));
      bool bad = !(big <= 3.0e38f) || (hh && !(o[8] <= SUM_LIMIT));
#pragma unroll
      for (int i = 0; i < 12; ++i) bad |= o[i] != o[i];
      if (__builtin_expect(__builtin_amdgcn_ballot_w64(bad) != 0, 0)) {
        const int qt = c * 8 + tt;
        const int lo = m ? 0 : (qt >= 8 ? qt - 8 : 0), hi = m ? 63 : qt;
        if (hh) { q2.x = 0u; q2.y = 0u; }
        float mx = -3.0e38f;
        for (int pass = 0; pass < 2; ++pass) {
          o = zero16;
          for (int kt = lo; kt <= hi; ++kt) {
            const unsigned so = kvl + (kt >> 3) * KV_CHUNK;
            u32x4 k1, v0, v1; u32x2 k2;
            unsigned spins = 0;
            do {
              k1 = ld128(rs, lane * 16 + (kt & 7) * 1536, so);
              k2 = ld64(rs, lane * 8 + (kt & 7) * 1536 + 1024, so);
              v0 = ld128(rs, KV_K + (kt & 7) * VT + vl, so);
              v1 = ld128(rs, KV_K + (kt & 7) * VT + 2 * VOCT + vl, so);
            } while (unwritten(umax4(umax4(umax4(umax(k2.x, k2.y), k1), v0), v1)) && !give_up(spins, tmo));
            f32x16 cin = zero16;
            if (!m && kt == qt) cin = tri_mask(t, hh, true);
            if (!m && kt == qt - 8) cin = tri_mask(t, hh, false);
            f32x16 s = mfma32k8(k2, q2, mfma32(k1, q1, cin));
            if (pass == 0) {
              mx = fmaxf(mx, max16(s));
            } else {
#pragma unroll
              for (int e = 0; e < 16; ++e) s[e] = __builtin_amdgcn_exp2f(s[e]);
              o = pv_lo(v0, s, o);
              o = pv_hi(v1, s, o);
            }
          }
          if (pass == 0) { mx = fmaxf(half_lo(mx), half_hi(mx)); set_shift(q2, mx, hh); }
        }
      }
      const float inv = __builtin_amdgcn_rcpf(half_hi(o[8]));
      const u32x4 p = {pack2(o[0] * inv, o[1] * inv), pack2(o[2] * inv, o[3] * inv), pack2(o[4] * inv, o[5] * inv), pack2(o[6] * inv, o[7] * inv)};
      const unsigned oo = OFF_O + layer * XIMG + (c * 16 + tt * 2) * 20480;
      const unsigned lo = (t >> 4) * 20480 + (t & 15) * 16;
      st128(clean4(p), rs, lo + hh * 256, oo + h * 512);
      st64(clean2((u32x2){pack2(o[8] * inv, o[9] * inv), pack2(o[10] * inv, o[11] * inv)}), rs, hh ? OOB : lo, oo + (64 + (h >> 1)) * 256 + (h & 1) * 8);
    }
    STAMP(6 + layer * 10);

    {
      LANE_VARS;
      const int cg = h & 7, t16 = lane & 15, g4 = lane >> 4, kh = w >> 2;
      const int row = c * 256 + (h >> 3) * 64 + (w & 3) * 16 + t16;
      const unsigned xl = (row >> 5) * XT + (row & 31) * 16 + cg * 5120;
      const unsigned xo = OFF_X + layer * XIM2;
      const u32x4 ra = ld128(rs, xl + g4 * 512, xo), rb = ld128(rs, xl + (4 + g4) * 512, xo);
      const u32x2 rc = ld64(rs, xl + (8 + (g4 >> 1)) * 512 + (g4 & 1) * 8, xo);
      const unsigned oo = OFF_O + layer * XIMG + (c * 16 + (h >> 3) * 4 + (w & 3)) * 20480 + kh * 10240;
      const unsigned ol = lane * 16;
#define OLD(k) ld128(rs, ol + ((k) & 3) * 1024, oo + ((k) >> 2) * 4096)
      u32x4 ob[10];
      bar_lds();
#pragma unroll
      for (int ks = 0; ks < 10; ++ks) ob[ks] = OLD(ks);
      SB();
      f32x4 acc[5];
#pragma unroll
      for (int n = 0; n < 5; ++n) acc[n] = (f32x4){0.f, 0.f, 0.f, 0.f};
      const unsigned wa0 = opaque(L_A + kh * 51200 + lane * 16);
      constexpr int WD = OWD;
      u32x4 wf[WD + 1];
#pragma unroll
      for (int i = 0; i < WD; ++i) wf[i] = lds_r128(wa0 + i * 1024);
#pragma unroll
      for (int g = 0; g < 2; ++g) {
        unsigned mx = 0;
#pragma unroll
        for (int i = 0; i < 5; ++i) mx = umax4(mx, ob[5 * g + i]);
        if (__builtin_expect(unwritten(mx), 0)) {
          unsigned spins = 0;
          do {
            if (give_up(spins, tmo)) break;
#pragma unroll
            for (int i = 5 * g; i < 10; ++i) ob[i] = OLD(i);
            mx = 0;
#pragma unroll
            for (int i = 0; i < 5; ++i) mx = umax4(mx, ob[5 * g + i]);
          } while (unwritten(mx));
        }
        if (g == 0) STAMP(7 + layer * 10);
#pragma unroll
        for (int e = 0; e < 25; ++e) {
          const int i = 25 * g + e, in = i + WD;
          if (in < 50) wf[in % (WD + 1)] = lds_r128(wa0 + in * 1024);
          acc[i % 5] = mfma16(wf[i % (WD + 1)], ob[i / 5], acc[i % 5]);
          SB();
        }
      }
      STAMP(8 + layer * 10);
      const unsigned sa = L_S + (w & 3) * 5120 + lane * 16;
      if (kh) {
#pragma unroll
        for (int n = 0; n < 5; ++n) *(LAS f32x4*)(sa + n * 1024) = acc[n];
      }
      bar_lds();
      if (!kh) {
#pragma unroll
        for (int n = 0; n < 5; ++n) acc[n] += *(LAS f32x4*)(sa + n * 1024);
        const u32x4 na = {pack2(bf_lo(ra.x) + acc[0][0], bf_hi(ra.x) + acc[0][1]), pack2(bf_lo(ra.y) + acc[0][2], bf_hi(ra.y) + acc[0][3]),
                          pack2(bf_lo(ra.z) + acc[1][0], bf_hi(ra.z) + acc[1][1]), pack2(bf_lo(ra.w) + acc[1][2], bf_hi(ra.w) + acc[1][3])};
        const u32x4 nb = {pack2(bf_lo(rb.x) + acc[2][0], bf_hi(rb.x) + acc[2][1]), pack2(bf_lo(rb.y) + acc[2][2], bf_hi(rb.y) + acc[2][3]),
                          pack2(bf_lo(rb.z) + acc[3][0], bf_hi(rb.z) + acc[3][1]), pack2(bf_lo(rb.w) + acc[3][2], bf_hi(rb.w) + acc[3][3])};
        const u32x2 nc = {pack2(bf_lo(rc.x) + acc[4][0], bf_hi(rc.x) + acc[4][1]), pack2(bf_lo(rc.y) + acc[4][2], bf_hi(rc.y) + acc[4][3])};
        if (layer < 3) {
          st128(clean4(na), rs, xl + g4 * 512, xo + XIM2);
          st128(clean4(nb), rs, xl + (4 + g4) * 512, xo + XIM2);
          st64(clean2(nc), rs, xl + (8 + (g4 >> 1)) * 512 + (g4 & 1) * 8, xo + XIM2);
          float s0 = sq2(na.x, 0.f), s1 = sq2(nb.x, 0.f);
          s0 = sq2(na.y, s0); s1 = sq2(nb.y, s1); s0 = sq2(na.z, s0); s1 = sq2(nb.z, s1);
          s0 = sq2(na.w, s0); s1 = sq2(nb.w, s1); s0 = sq2(nc.x, s0); s1 = sq2(nc.y, s1);
          s0 += s1;
          s0 = half_lo(s0) + half_hi(s0);
          const auto q16 = __builtin_amdgcn_permlane16_swap(__float_as_uint(s0), __float_as_uint(s0), false, false);
          s0 = __uint_as_float(q16[0]) + __uint_as_float(q16[1]);
          __builtin_amdgcn_raw_buffer_store_b32(clean(__float_as_uint(s0)), rs, lane < 16 ? (row >> 5) * XT + 40960 + (row & 31) * 4 + cg * 128 : OOB, xo + XIM2, 16);
        } else {
          const unsigned od = row * 1280 + cg * 160;
          st128a<0>(na, ro, od + g4 * 16, 0);
          st128a<0>(nb, ro, od + 64 + g4 * 16, 0);
          __builtin_amdgcn_raw_buffer_store_b64(nc, ro, od + 128 + g4 * 8, 0, 0);
        }
      }
      bar_lds();
    }
    STAMP(9 + layer * 10);
  }
}

extern "C" void kernel_launch(void* const* d_in, const int* in_sizes, int n_in, void* d_out, int out_size, void* d_ws, size_t ws_size,
                              hipStream_t stream) {
  (void)in_sizes; (void)n_in; (void)out_size; (void)ws_size;
  (void)hipFuncSetAttribute((const void*)main_kernel, hipFuncAttributeMaxDynamicSharedMemorySize, LDS_BYTES);
  setup_kernel<<<PB_TOTAL, 512, 0, stream>>>((const u16*)d_in[0], (const u16*)d_in[1], (const u16*)d_in[2], (const u16*)d_in[4],
                                             (const u16*)d_in[5], (char*)d_ws);
  main_kernel<<<256, 768, LDS_BYTES, stream>>>((char*)d_ws, (u16*)d_out);
}
```

```cpp
#include <hip/hip_runtime.h>
#include <hip/hip_bf16.h>

typedef float f32x16 __attribute__((ext_vector_type(16)));
typedef float f32x8 __attribute__((ext_vector_type(8)));
typedef float f32x4 __attribute__((ext_vector_type(4)));
typedef unsigned u32x4 __attribute__((ext_vector_type(4)));
typedef unsigned u32x2 __attribute__((ext_vector_type(2)));
typedef __bf16 bf16x16 __attribute__((ext_vector_type(16)));
typedef __bf16 bf16x8 __attribute__((ext_vector_type(8)));
typedef __bf16 bf16x2 __attribute__((ext_vector_type(2)));
typedef short s16x4 __attribute__((ext_vector_type(4)));
typedef unsigned short u16;

#define LAS __attribute__((address_space(3)))
#define GAS __attribute__((address_space(1)))
typedef unsigned long long u64;
typedef GAS u64 gu64;
#define RLX_AGENT __ATOMIC_RELAXED, __HIP_MEMORY_SCOPE_AGENT
#define DEVI __device__ __forceinline__

constexpr unsigned WQ_HEAD = 81920;
constexpr unsigned WO_CG = 102400;
constexpr unsigned XIMG = 2621440;
constexpr unsigned XT = 43008, XIM2 = 64 * XT;
constexpr unsigned VOCT = 352;
constexpr unsigned TILE = 2944, T_K2 = 1024, T_V = 1536;
constexpr unsigned UNIT = 4 * TILE;
constexpr unsigned KV_LAYER = 32 * 64 * TILE;
constexpr unsigned OFF_FLAG = 0;
constexpr unsigned OFF_F1 = 4096, OFF_F2 = OFF_F1 + 32768;
constexpr unsigned OFF_KF = OFF_F2 + 32768;
constexpr unsigned OFF_WQ = OFF_KF + 4 * 32 * 16 * 128;
constexpr unsigned OFF_WO = OFF_WQ + 2 * 32 * WQ_HEAD;
constexpr unsigned OFF_X = OFF_WO + 2 * 8 * WO_CG;
constexpr unsigned OFF_O = OFF_X + 4 * XIM2;
constexpr unsigned OFF_KV = OFF_O + 4 * XIMG;
constexpr unsigned WS_USED = OFF_KV + 4 * KV_LAYER;
constexpr unsigned OFF_DBG = WS_USED;
constexpr unsigned SENT = 0xFFFFFFFFu;
constexpr unsigned OOB = 0xE0000000u;

constexpr unsigned L_A = 0;
constexpr unsigned L_U = 102400;
constexpr unsigned L_M = L_U + 5 * UNIT;
constexpr unsigned L_C = L_M + 512;
constexpr unsigned LDS_BYTES = 163840;
enum { C_RDY = 0, C_DONE = 16, C_WO = 32, C_OP = 33, C_WQ = 34, C_RED = 38, C_XS = 42, C_GO = 46, C_PR = 47, C_W0 = 48, C_X0 = 49, C_INIT = 50, C_SEEN = 51, C_CS = 52, C_PUB = 53, C_F1 = 54, C_OL = 55, C_OS = 56 };

constexpr unsigned kSpinCap = 1u << 15;
#define NODOT 0
#define ABL 0
#define XPG 2
#define OWD 16
#define HPRIO 3
#define WIN 10
#define TOUCH 0
#define YPRI 2
#define ONAP 2
#define HNAP 2
#define GPRI 1
#define OLN 8
#define PREF 1
#define RWIN 3
#define XI0 8
#define XD 12
#define OSN 8
#define ODRAIN 0
#define OSLP 6
#define XSLP 0
#define WOE 40
#define GOAT 36
#define XDRAIN 0
#define PACE 3
#define XPD 16
#define MAPC 2
#define SUM_LIMIT 1.0e18f

DEVI unsigned pack2(float a, float b) {
  bf16x2 v = {(__bf16)a, (__bf16)b};
  return __builtin_bit_cast(unsigned, v);
}
DEVI float sq2(unsigned u, float acc) {
  const bf16x2 p = __builtin_bit_cast(bf16x2, u);
  return __builtin_amdgcn_fdot2_f32_bf16(p, p, acc, false);
}
DEVI float bf_lo(unsigned u) { return __uint_as_float(u << 16); }
DEVI float bf_hi(unsigned u) { return __uint_as_float(u & 0xffff0000u); }
DEVI f32x16 mfma32(u32x4 a, u32x4 b, f32x16 c) {
  return __builtin_amdgcn_mfma_f32_32x32x16_bf16(__builtin_bit_cast(bf16x8, a), __builtin_bit_cast(bf16x8, b), c, 0, 0, 0);
}
DEVI f32x16 mfma32k8(u32x2 a, u32x2 b, f32x16 c) {
  return __builtin_amdgcn_mfma_f32_32x32x8bf16_1k(__builtin_bit_cast(s16x4, a), __builtin_bit_cast(s16x4, b), c, 0, 0, 0);
}
DEVI f32x4 mfma16(u32x4 a, u32x4 b, f32x4 c) {
  return __builtin_amdgcn_mfma_f32_16x16x32_bf16(__builtin_bit_cast(bf16x8, a), __builtin_bit_cast(bf16x8, b), c, 0, 0, 0);
}
DEVI f32x16 pv_lo(u32x4 v, const f32x16& p, f32x16 o) {
  f32x8 h = __builtin_shufflevector(p, p, 0, 1, 2, 3, 4, 5, 6, 7);
  return __builtin_amdgcn_smfmac_f32_32x32x32_bf16(__builtin_bit_cast(bf16x8, v), __builtin_bit_cast(bf16x16, h), o, 0xDDDD, 0, 0);
}
DEVI f32x16 pv_hi(u32x4 v, const f32x16& p, f32x16 o) {
  f32x8 h = __builtin_shufflevector(p, p, 8, 9, 10, 11, 12, 13, 14, 15);
  return __builtin_amdgcn_smfmac_f32_32x32x32_bf16(__builtin_bit_cast(bf16x8, v), __builtin_bit_cast(bf16x16, h), o, 0xDDDD, 0, 0);
}
DEVI u32x4 lds_r128(unsigned a) { return *(LAS u32x4*)a; }
DEVI u32x2 lds_r64(unsigned a) { return *(LAS u32x2*)a; }
DEVI void lds_w128(unsigned a, u32x4 v) { *(LAS u32x4*)a = v; }
DEVI void lds_w64(unsigned a, u32x2 v) { *(LAS u32x2*)a = v; }
DEVI void lds_w16(unsigned a, float v) { *(LAS __bf16*)a = (__bf16)v; }
DEVI float half_lo(float v) { return __uint_as_float(__builtin_amdgcn_permlane32_swap(__float_as_uint(v), __float_as_uint(v), false, false)[0]); }
DEVI float half_hi(float v) { return __uint_as_float(__builtin_amdgcn_permlane32_swap(__float_as_uint(v), __float_as_uint(v), false, false)[1]); }
DEVI unsigned umax(unsigned a, unsigned b) { return a > b ? a : b; }
DEVI unsigned umax4(unsigned m, u32x4 v) { return umax(umax(umax(umax(m, v.x), v.y), v.z), v.w); }
DEVI bool unwritten(unsigned m) { return __builtin_amdgcn_ballot_w64(m == SENT) != 0; }
DEVI unsigned clean(unsigned v) { return v < SENT ? v : SENT - 1; }
DEVI u32x4 clean4(u32x4 v) { return (u32x4){clean(v.x), clean(v.y), clean(v.z), clean(v.w)}; }
DEVI u32x2 clean2(u32x2 v) { return (u32x2){clean(v.x), clean(v.y)}; }
DEVI unsigned vpos(int d) {
  const unsigned q = ((14u | 0u << 5 | 4u << 10 | 18u << 15 | 8u << 20) >> ((d >> 2) * 5 & 31)) & 31u;
  return d < 20 ? q + (d & 3) : (d == 20 ? 13u : 12u);
}
DEVI void lds_w16lo(unsigned a, unsigned v) { *(LAS u16*)a = (u16)v; }
DEVI void lds_w16hi(unsigned a, unsigned v) { *(LAS u16*)a = (u16)(v >> 16); }
DEVI float sum2(unsigned u, float acc) {
  const bf16x2 ones = {(__bf16)1.0f, (__bf16)1.0f};
  return __builtin_amdgcn_fdot2_f32_bf16(__builtin_bit_cast(bf16x2, u), ones, acc, false);
}
DEVI void split3(float v, unsigned& hi, unsigned& mid, unsigned& lo) {
  hi = __float_as_uint(v) & 0xffff0000u;
  const float r1 = v - __uint_as_float(hi);
  mid = __float_as_uint(r1) & 0xffff0000u;
  lo = __float_as_uint(r1 - __uint_as_float(mid)) & 0xffff0000u;
}
#define SB() __builtin_amdgcn_sched_barrier(0)
DEVI void bar_lds() {
  asm volatile("s_waitcnt lgkmcnt(0)\n\ts_barrier" ::: "memory");
}
DEVI void drain_vm() { asm volatile("s_waitcnt vmcnt(0)" ::: "memory"); }
#define DMA_ASM(d, v, r, o) "s_mov_b32 m0, " d "\n\ts_nop 3\n\tbuffer_load_dwordx4 " v ", " r ", " o " offen sc1 lds\n\t"
DEVI void dma1k(unsigned lds_dst, unsigned voff, u32x4 srd, unsigned soff) {
  unsigned keep;
  asm volatile("s_mov_b32 %0, m0\n\t" DMA_ASM("%1", "%2", "%3", "%4") "s_mov_b32 m0, %0"
               : "=&s"(keep) : "s"(__builtin_amdgcn_readfirstlane(lds_dst)), "v"(voff), "s"(srd), "s"(__builtin_amdgcn_readfirstlane(soff)) : "memory");
}
constexpr unsigned NO_LDS = 0x30000;
typedef __amdgpu_buffer_rsrc_t rsrc_t;
DEVI u32x4 ld128(rsrc_t r, unsigned voff, unsigned soff) { return __builtin_amdgcn_raw_buffer_load_b128(r, voff, soff, 16); }
DEVI u32x2 ld64(rsrc_t r, unsigned voff, unsigned soff) { return __builtin_amdgcn_raw_buffer_load_b64(r, voff, soff, 16); }
template <int AUX> DEVI void st128a(u32x4 v, rsrc_t r, unsigned voff, unsigned soff) {
  __builtin_amdgcn_raw_buffer_store_b128(v, r, voff, soff, AUX);
  asm volatile("s_nop 1" :: "v"(v) : "memory");
}
DEVI void st128(u32x4 v, rsrc_t r, unsigned voff, unsigned soff) { st128a<16>(v, r, voff, soff); }
DEVI void st64(u32x2 v, rsrc_t r, unsigned voff, unsigned soff) { __builtin_amdgcn_raw_buffer_store_b64(v, r, voff, soff, 16); }

DEVI bool give_up(unsigned& spins) {
  __builtin_amdgcn_s_sleep(2);
  return ++spins > kSpinCap;
}
DEVI u32x2 next_tag(u32x2 g) {
  u64 v = ((u64)g.y << 32 | g.x) + 1;
  v += v == ~0ull;
  return (u32x2){(unsigned)v, (unsigned)(v >> 32)};
}
DEVI void wait_tag(rsrc_t rs, unsigned off, u32x2 tag) {
  for (unsigned spins = 0;;) {
    const u32x2 v = ld64(rs, off, 0);
    if (__builtin_amdgcn_ballot_w64(off != OOB && (v.x != tag.x || v.y != tag.y)) == 0 || give_up(spins)) break;
  }
  asm volatile("" ::: "memory");
}

#define STAMP(i) do {} while (0)
#define STAMP_OUT() do {} while (0)

DEVI void transpose8x4(const u32x2 (&r)[8], u32x4 (&o)[4]) {
#pragma unroll
  for (int p = 0; p < 4; ++p) {
    o[0][p] = __builtin_amdgcn_perm(r[2 * p + 1].x, r[2 * p].x, 0x05040100u);
    o[1][p] = __builtin_amdgcn_perm(r[2 * p + 1].x, r[2 * p].x, 0x07060302u);
    o[2][p] = __builtin_amdgcn_perm(r[2 * p + 1].y, r[2 * p].y, 0x05040100u);
    o[3][p] = __builtin_amdgcn_perm(r[2 * p + 1].y, r[2 * p].y, 0x07060302u);
  }
}

DEVI void wq_item_load(u32x2 (&r)[8], rsrc_t rw, int h, int kh, int qd, bool on) {
  const int mt = qd >> 3, rq = qd & 7;
  const int col = h * 20 + (rq < 5 ? mt * 640 + 4 * rq : 1280 + mt * 12 + 4 * (rq - 5));
  const unsigned off = (on && qd < 15) ? kh * 8 * 3840 + col * 2 : OOB;
#pragma unroll
  for (int j = 0; j < 8; ++j) r[j] = __builtin_amdgcn_raw_buffer_load_b64(rw, off, j * 3840, 0);
}
DEVI unsigned wq_item_pos(int kh, int qd) { return (kh >> 1) * 2048 + (qd >> 3) * 1024 + (kh & 1) * 512 + (qd & 7) * 64; }

struct Frag { u32x4 k1; u32x2 k2; u32x4 va, vb; };

template <bool QK, bool LDK, bool LDV>
DEVI void att_step(f32x16& cur, f32x16& oth, f32x16& o, Frag& f, const u32x4& q1, const u32x2& q2, const f32x16& cin,
                   unsigned a_vb, unsigned a_k1, unsigned a_k2, unsigned a_va) {
  o = pv_hi(f.vb, oth, o);
  f.vb = lds_r128(a_vb);
  SB();
#pragma unroll
  for (int i = 0; i < 4; ++i) cur[i] = __builtin_amdgcn_exp2f(cur[i]);
  SB();
  if (QK) oth = mfma32(f.k1, q1, cin);
  if (LDK) f.k1 = lds_r128(a_k1);
  SB();
#pragma unroll
  for (int i = 4; i < 8; ++i) cur[i] = __builtin_amdgcn_exp2f(cur[i]);
  SB();
  if (QK) oth = mfma32k8(f.k2, q2, oth);
  if (LDK) f.k2 = lds_r64(a_k2);
  SB();
#pragma unroll
  for (int i = 8; i < 12; ++i) cur[i] = __builtin_amdgcn_exp2f(cur[i]);
  SB();
  o = pv_lo(f.va, cur, o);
  if (LDV) f.va = lds_r128(a_va);
  SB();
#pragma unroll
  for (int i = 12; i < 16; ++i) cur[i] = __builtin_amdgcn_exp2f(cur[i]);
  SB();
}
DEVI void bar_if_eq(int a, int b) {
  asm volatile("s_cmp_lg_u32 %0, %1\n\ts_cbranch_scc1 .Lskip%=\n\ts_waitcnt lgkmcnt(0)\n\ts_barrier\n.Lskip%=:" :: "s"(a), "s"(b) : "memory", "scc");
}
DEVI void bar_if_ne(int a, int b) {
  asm volatile("s_cmp_eq_u32 %0, %1\n\ts_cbranch_scc1 .Lskip%=\n\ts_waitcnt lgkmcnt(0)\n\ts_barrier\n.Lskip%=:" :: "s"(a), "s"(b) : "memory", "scc");
}
DEVI void prio_if(int on) {
  asm volatile("s_cmp_eq_u32 %0, 0\n\ts_cbranch_scc1 .Lskip%=\n\ts_setprio 1\n.Lskip%=:" :: "s"(on) : "scc");
}
DEVI int opaque(int v) { asm volatile("" : "+v"(v)); return v; }

DEVI float max16(const f32x16& s) {
  float m = fmaxf(fmaxf(s[0], s[1]), s[2]);
#pragma unroll
  for (int i = 3; i < 15; i += 2) m = fmaxf(fmaxf(m, s[i]), s[i + 1]);
  return fmaxf(m, s[15]);
}
DEVI void set_shift(u32x2& q2, float m, int hh) {
  unsigned hi, mid, lo;
  split3(-m, hi, mid, lo);
  q2.x = hh ? (hi >> 16) | mid : q2.x;
  q2.y = hh ? (lo >> 16) | 0xFF7F0000u : q2.y;
}
template <bool DIAG> DEVI f32x16 tri_mask() {
  f32x16 c;
#pragma unroll
  for (int i = 0; i < 16; ++i) {
    const int key = (i & 3) + 8 * (i >> 2);
    const u64 above = ((1ull << key) - 1) | ((1ull << (key + 4)) - 1) << 32;
    c[i] = __builtin_amdgcn_inverse_ballot_w64(DIAG ? above : ~above) ? -3.0e38f : 0.f;
  }
  return c;
}

DEVI void ev_wait_if_eq(int a, int b, unsigned addr, unsigned target) {
  unsigned sv, cnt, v;
  asm volatile("s_cmp_lg_u32 %3, %4\n\ts_cbranch_scc1 .Lgo%=\n\ts_mov_b32 %1, 0x100000\n\ts_nop 7\n\ts_nop 3\n"
               ".Lspin%=:\n\tds_read_b32 %2, %5\n\ts_waitcnt lgkmcnt(0)\n\tv_readfirstlane_b32 %0, %2\n\t"
               "s_cmp_ge_u32 %0, %6\n\ts_cbranch_scc1 .Lgo%=\n\ts_sleep 1\n\ts_sub_u32 %1, %1, 1\n\ts_cbranch_scc0 .Lspin%=\n"
               ".Lgo%=:"
               : "=&s"(sv), "=&s"(cnt), "=&v"(v) : "s"(a), "s"(b), "v"(addr), "s"(target) : "memory", "scc");
}
DEVI void ev_post(unsigned addr, unsigned n = 1) {
  asm volatile("s_mov_b64 exec, 1\n\tds_add_u32 %0, %1\n\ts_mov_b64 exec, -1" :: "v"(addr), "v"(n) : "memory");
}
DEVI unsigned ev_peek(unsigned addr) { return *(volatile LAS unsigned*)addr; }
template <int NAP = 1> DEVI void ev_wait_from(unsigned seen, unsigned addr, unsigned target) {
  unsigned sv, cnt;
  asm volatile("s_nop 0\n\tv_readfirstlane_b32 %0, %2\n\ts_cmp_ge_u32 %0, %4\n\ts_cbranch_scc1 .Lgo%=\n\t"
               "s_mov_b32 %1, 0x100000\n"
               ".Lspin%=:\n\ts_sleep %5\n\tds_read_b32 %2, %3\n\ts_sub_u32 %1, %1, 1\n\ts_cbranch_scc1 .Lout%=\n\t"
               "s_waitcnt lgkmcnt(0)\n\tv_readfirstlane_b32 %0, %2\n\t"
               "s_cmp_ge_u32 %0, %4\n\ts_cbranch_scc0 .Lspin%=\n"
               ".Lout%=:\n\ts_waitcnt lgkmcnt(0)\n"
               ".Lgo%=:"
               : "=&s"(sv), "=&s"(cnt), "+&v"(seen)
               : "v"(addr), "s"(__builtin_amdgcn_readfirstlane(target)), "i"(NAP) : "memory", "scc");
}
template <int NAP = 1> DEVI void ev_wait(unsigned addr, unsigned target) { ev_wait_from<NAP>(ev_peek(addr), addr, target); }

DEVI void kv_publish(unsigned l0, unsigned l5, rsrc_t rs, unsigned un, unsigned slot) {
  u32x4 g[6];
#pragma unroll
  for (int i = 0; i < 6; ++i) g[i] = lds_r128(L_U + slot * UNIT + l0 + i * 2048);
#pragma unroll
  for (int i = 0; i < 6; ++i) st128(g[i], rs, i < 5 ? l0 : l5, OFF_KV + un * UNIT + i * 2048);
}
DEVI void kv_fetch(unsigned l16, u32x4 srd, unsigned un, unsigned slot, int first, int step) {
  for (int i = first; i < 12; i += step) {
    const unsigned o = i < 11 ? i * 1024 : UNIT - 1024;
    dma1k(L_U + slot * UNIT + o, l16, srd, OFF_KV + un * UNIT + o);
  }
}
DEVI void kv_fake(unsigned l0, unsigned l5, unsigned slot) {
#pragma unroll
  for (int i = 0; i < 6; ++i) {
    const unsigned o = (l0 + i * 2048) % TILE, one = (o >= 1280 && o < T_V) ? 0x3F800000u : 0u;
    if (i < 5 || l5 != OOB) lds_w128(L_U + slot * UNIT + l0 + i * 2048, (u32x4){0u, one, 0u, one});
  }
}
DEVI unsigned reset_piece(unsigned q, int l, int c, int h) {
  if (q < 20) return OFF_O + l * XIMG + (q < 10 ? q * 1024 : XIMG + (q - 10) * 1024) + c * 327680 + h * 10240;
  const unsigned k = q < 31 ? q - 20 : q - 31;
  return OFF_X + (q < 31 ? l + 1 : l) * XIM2 + c * 8 * XT + h * (XT / 4) + (k < 10 ? k * 1024 : XT / 4 - 1024);
}
DEVI void add_colsum(const u32x4 (&wn)[20], int u, int lane, unsigned dst) {
  float s0 = 0.f, s1 = 0.f;
  const bf16x2 ones = {(__bf16)1.0f, (__bf16)1.0f};
#pragma unroll
  for (int i = 0; i < 20; ++i) {
    const unsigned a = wn[i].x, b = wn[i].y, c = wn[i].z, d = wn[i].w;
    s0 = __builtin_amdgcn_fdot2_f32_bf16(__builtin_bit_cast(bf16x2, a), ones, s0, false);
    s1 = __builtin_amdgcn_fdot2_f32_bf16(__builtin_bit_cast(bf16x2, b), ones, s1, false);
    s0 = __builtin_amdgcn_fdot2_f32_bf16(__builtin_bit_cast(bf16x2, c), ones, s0, false);
    s1 = __builtin_amdgcn_fdot2_f32_bf16(__builtin_bit_cast(bf16x2, d), ones, s1, false);
  }
  s0 += s1;
  s0 = half_lo(s0) + half_hi(s0);
  if (lane < 32) __hip_atomic_fetch_add((LAS float*)(dst + (u & 1) * 128 + lane * 4), s0, __ATOMIC_RELAXED, __HIP_MEMORY_SCOPE_WORKGROUP);
}

DEVI void weight_share(int task, int lane, int c, int h, const u16* wq, const u16* wo, rsrc_t rs, unsigned bounce, bool bar) {
  const int m2 = task >> 2, wi = task & 3;
  u32x2 r[8];
  u32x4 o[4];
  unsigned dst, pos, np;
  if (wi < 3) {
    const rsrc_t rw = __builtin_amdgcn_make_buffer_rsrc((void*)wq, 0, 640 * 3840, 0x00020000);
    wq_item_load(r, rw, h, c * 10 + wi * 4 + (lane >> 4), lane & 15, wi < 2 || lane < 32);
    pos = wq_item_pos(lane >> 4, lane & 15);
    dst = OFF_WQ + (m2 * 32 + h) * WQ_HEAD + (c * 10 + wi * 4) * 1024;
    np = wi < 2 ? 4 : 2;
  } else {
    const int b32 = (h >> 3) * 8 + c, p = b32 * 4 + (lane >> 4), nt = p % 5, q = (p / 5) * 4 + ((lane >> 2) & 3), g = lane & 3;
    const int c4 = nt < 4 ? (nt >> 1) * 8 + g * 2 + (nt & 1) : 16 + g;
    const rsrc_t rw = __builtin_amdgcn_make_buffer_rsrc((void*)wo, 0, 640 * 1280, 0x00020000);
    const int k0 = q < 64 ? (q >> 1) * 20 + (q & 1) * 4 : (q - 64) * 40 + 16, dk = q < 64 ? 8 : 20;
    const unsigned wof = b32 < 25 ? k0 * 1280 + ((h & 7) * 80 + c4 * 4) * 2 : OOB;
#pragma unroll
    for (int j = 0; j < 4; ++j) {
      r[j] = __builtin_amdgcn_raw_buffer_load_b64(rw, wof, j * 1280, 0);
      r[4 + j] = __builtin_amdgcn_raw_buffer_load_b64(rw, b32 < 25 ? wof + dk * 1280 : OOB, j * 1280, 0);
    }
    pos = lane * 64;
    dst = OFF_WO + (m2 * 8 + (h & 7)) * WO_CG + b32 * 4096;
    np = b32 < 25 ? 4 : 0;
  }
  if (bar) bar_lds();
  transpose8x4(r, o);
#pragma unroll
  for (int i = 0; i < 4; ++i) lds_w128(bounce + pos + i * 16, o[i]);
#pragma unroll
  for (int i = 0; i < 4; ++i) o[i] = lds_r128(bounce + i * 1024 + lane * 16);
#pragma unroll
  for (int i = 0; i < 4; ++i) st128(o[i], rs, (unsigned)i < np ? lane * 16 : OOB, dst + i * 1024);
}

__global__ void __launch_bounds__(768, 3) main_kernel(const u16* __restrict__ x, const u16* __restrict__ wqA, const u16* __restrict__ woA,
                                                      const u16* __restrict__ wqB, const u16* __restrict__ woB,
                                                      char* __restrict__ ws, u16* __restrict__ out) {
  const int tid0 = threadIdx.x;
  const int w = __builtin_amdgcn_readfirstlane(tid0 >> 6);
  const int y = (w >> 2) & 1;
  const int tt = y ? 7 - w : w + 4;
  const int xg = blockIdx.x & 7, xi = blockIdx.x >> 3;
  const int c = ((xg << MAPC) & 7) | (xi & ((1 << MAPC) - 1));
  const int h = ((xg >> (3 - MAPC)) << (5 - MAPC)) | (xi >> MAPC);
#define LANE_VARS const int tid = opaque(tid0), lane = tid & 63, t = lane & 31, hh = lane >> 5; (void)t; (void)hh
  const rsrc_t rs = __builtin_amdgcn_make_buffer_rsrc(ws, 0, WS_USED, 0x00020000);
  const rsrc_t ro = __builtin_amdgcn_make_buffer_rsrc(out, 0, XIMG, 0x00020000);
  const u32x4 srd = {(unsigned)(size_t)ws, (unsigned)((size_t)ws >> 32) & 0xffffu, WS_USED, 0x00020000u};
  const f32x16 zero16 = {0.f, 0.f, 0.f, 0.f, 0.f, 0.f, 0.f, 0.f, 0.f, 0.f, 0.f, 0.f, 0.f, 0.f, 0.f, 0.f};
  STAMP(0);
  if (tid0 < 384) *(LAS unsigned*)(L_M + tid0 * 4) = 0u;

  if (w >= 8) {
    const int u = w - 8, lane = tid0 & 63, me = h * 8 + c;
    const unsigned l16 = lane * 16, l4 = u * 1024 + l16;
    const unsigned l0 = (u & 1) * 1024 + l16, l5 = l0 + 10240 < UNIT ? l0 : OOB;
    const u32x4 sv = {SENT, SENT, SENT, SENT};
    __builtin_amdgcn_s_setprio(HPRIO);
    u32x2 tag = ld64(rs, 0, OFF_FLAG);
    if (u == 0) {
      const rsrc_t rx = __builtin_amdgcn_make_buffer_rsrc((void*)x, 0, XIMG, 0x00020000);
      const int row = c * 256 + h * 8 + (lane & 7), p = lane >> 3;
      u32x4 v[10];
#pragma unroll
      for (int i = 0; i < 10; ++i) v[i] = __builtin_amdgcn_raw_buffer_load_b128(rx, row * 1280 + p * 16, i * 128, 0);
      st128(sv, rs, lane < 16 ? (p ? OFF_F2 : OFF_F1) + me * 128 + (lane & 7) * 16 : OOB, 0);
      st128(sv, rs, OFF_KF + (((p >> 1) * 32 + h) * 16 + 2 * c + (p & 1)) * 128 + (lane & 7) * 16, 0);
      bar_lds();
      const unsigned xd = OFF_X + (row >> 5) * XT;
      float ss = 0.f, sm = 0.f;
#pragma unroll
      for (int i = 0; i < 10; ++i) {
        st128(clean4(v[i]), rs, xd + p * 512 + (row & 31) * 16, i * 4096);
        ss = sq2(v[i].x, ss); ss = sq2(v[i].y, ss); ss = sq2(v[i].z, ss); ss = sq2(v[i].w, ss);
        sm = sum2(v[i].x, sm); sm = sum2(v[i].y, sm); sm = sum2(v[i].z, sm); sm = sum2(v[i].w, sm);
      }
      st64(clean2((u32x2){__float_as_uint(sm), __float_as_uint(ss)}), rs, xd + 40960 + (row & 31) * 8 + p * 256, 0);
      tag = next_tag(tag);
      drain_vm();
      ev_wait(L_C + C_W0 * 4, 3);
      st64(tag, rs, lane == 0 ? 0u : OOB, OFF_F1 + me * 128);
      ev_post(L_C + C_F1 * 4);
      STAMP(41);
      wait_tag(rs, lane < 40 ? OFF_F1 + (lane < 32 ? lane * 8 + c : h * 8 + lane - 32) * 128 : OOB, tag);
      ev_post(L_C + C_X0 * 4);
      STAMP(42);
    } else {
      for (unsigned q = u - 1; q < 31; q += 3) st128(sv, rs, l16, reset_piece(q, 0, c, h));
      bar_lds();
      tag = next_tag(tag);
      ev_wait(L_C + C_X0 * 4, 1);
    }
    {
      u32x4 wn[20];
#pragma unroll
      for (int i = 0; i < 20; ++i) wn[i] = ld128(rs, l4, OFF_WQ + h * WQ_HEAD + i * 4096);
#pragma unroll
      for (int q = 0; q < 4; ++q) {
#pragma unroll
        for (int i = 5 * q; i < 5 * q + 5; ++i) lds_w128(L_A + l4 + i * 4096, wn[i]);
        ev_post(L_C + (C_WQ + q) * 4);
      }
      STAMP(45);
      add_colsum(wn, u, lane, L_M);
      ev_post(L_C + C_CS * 4);
      ev_post(L_C + C_INIT * 4);
      if (u == 0) {
        ev_wait<2>(L_C + C_INIT * 4, 12);
        st64(tag, rs, lane == 0 ? 0u : OOB, OFF_F2 + me * 128);
        drain_vm();
        STAMP(43);
        wait_tag(rs, OFF_F2 + (lane < 32 ? lane * 8 + c : (((lane >> 3) & 3) * 8 + (h & 7)) * 8 + (lane & 7)) * 128, tag);
        ev_post(L_C + C_SEEN * 4);
        STAMP(44);
      }
    }
    for (int layer = 0; layer < 4; ++layer) {
      const int m = layer & 1;
      const unsigned ev = L_C + layer * 256;
      const unsigned ub = (layer * 32 + h) * 16, wo_src = OFF_WO + (m * 8 + (h & 7)) * WO_CG;
      const unsigned hi = u < 2;
      const unsigned own = m ? 1 - hi : 3 + hi;
      const unsigned wq_next = layer < 3 ? OFF_WQ + ((m ^ 1) * 32 + h) * WQ_HEAD : OOB;
      if (!hi && m == 0) {
        ev_wait<2>(ev + C_PR * 4, 8);
        ev_wait(L_C + C_SEEN * 4, 1);
        for (int p = u - 2; p < WOE; p += 2) dma1k(L_A + p * 1024, l16, srd, wo_src + p * 1024);
      }
      ev_wait(ev + (C_RDY + own) * 4, 4);
      STAMP(1 + layer * 10);
      kv_publish(l0, l5, rs, ub + 2 * c + hi, own);
      drain_vm();
      st64(tag, rs, lane == 0 ? (u & 1) * 8 : OOB, OFF_KF + (ub + 2 * c + hi) * 128);
      ev_post(ev + C_PUB * 4);
      STAMP(2 + layer * 10);
      {
        const unsigned un = ub + 2 * ((c - 1) & 7) + hi, slot = m ? 3 - hi : 1 + hi;
        if (m || c > 0) {
          wait_tag(rs, lane < 2 ? OFF_KF + un * 128 + lane * 8 : OOB, tag);
          kv_fetch(l16, srd, un, slot, u & 1, 2);
          drain_vm();
        } else {
          kv_fake(l0, l5, slot);
        }
        ev_wait(L_C + C_SEEN * 4, 1);
        ev_post(ev + (C_RDY + slot) * 4, 2);
      }
      STAMP(3 + layer * 10);
      if (m == 0) {
        if (hi) {
          ev_wait(ev + (C_RDY + 3) * 4, 4);
          for (int p = WOE + u; p < 100; p += 2) dma1k(L_A + p * 1024, l16, srd, wo_src + p * 1024);
        }
      } else {
        ev_wait(ev + C_PUB * 4, 4);
        for (int n = 4 + u; n < 16; n += 4) {
          const unsigned un2 = ub + 2 * ((c - (n >> 1)) & 7) + 1 - (n & 1);
          wait_tag(rs, lane < 2 ? OFF_KF + un2 * 128 + lane * 8 : OOB, tag);
          if (n >= 5) ev_wait<HNAP>(ev + (C_DONE + n - 5) * 4, 8);
          kv_fetch(l16, srd, un2, n % 5, 0, 1);
          drain_vm();
          ev_post(ev + (C_RDY + n) * 4, 4);
          if (n < 8) {
            for (int p = u; p < 100; p += 4) { dma1k(L_A + p * 1024, l16, srd, wo_src + p * 1024); __builtin_amdgcn_s_sleep(1); }
          } else if (n < 12 && layer == 1) {
            for (unsigned q = u; q < 42; q += 4) { st128(sv, rs, l16, reset_piece(q, 2, c, h)); __builtin_amdgcn_s_sleep(2); }
          }
        }
        if (layer == 3) {
          ev_wait<4>(ev + (C_DONE + 15) * 4, 1);
          st64(tag, rs, (tid0 == 512 && blockIdx.x == 0) ? 0u : OOB, OFF_FLAG);
        }
      }
      drain_vm();
      ev_post(ev + C_WO * 4);
      STAMP(6 + layer * 10);
      if (layer == 3) break;
      if (m == 0) ev_wait<2>(ev + C_OL * 4, OLN);
      u32x4 wn[20];
#pragma unroll
      for (int i = 0; i < 20; ++i) wn[i] = ld128(rs, l4, wq_next + i * 4096);
      SB();
      if (layer == 0) { add_colsum(wn, u, lane, L_M + 256); ev_post(ev + 256 + C_CS * 4); }
      STAMP(7 + layer * 10);
      if (m) ev_wait<16>(ev + (C_DONE + 15) * 4, 4);
      ev_wait(ev + C_OP * 4, 4);
      STAMP(8 + layer * 10);
#pragma unroll
      for (int q = 0; q < 4; ++q) {
#pragma unroll
        for (int i = 5 * q; i < 5 * q + 5; ++i) lds_w128(L_A + l4 + i * 4096, wn[i]);
        ev_post(ev + 256 + (C_WQ + q) * 4);
      }
      STAMP(9 + layer * 10);
    }
    STAMP_OUT();
    return;
  }

  {
    const int lane = tid0 & 63;
    if (w < 3) {
      weight_share(w, lane, c, h, wqA, woA, rs, L_U + w * 4096, true);
      drain_vm();
      ev_post(L_C + C_W0 * 4);
    } else {
      const unsigned id = (blockIdx.x * 5 + w - 3) * 64 + lane;
      const rsrc_t r1 = __builtin_amdgcn_make_buffer_rsrc((void*)wqB, 0, 640 * 3840, 0x00020000);
      const rsrc_t r2 = __builtin_amdgcn_make_buffer_rsrc((void*)woA, 0, 640 * 1280, 0x00020000);
      const rsrc_t r3 = __builtin_amdgcn_make_buffer_rsrc((void*)woB, 0, 640 * 1280, 0x00020000);
      if (PREF) {
        const u32x4 p0 = __builtin_amdgcn_raw_buffer_load_b128(r1, id * 16, 0, 0), p1 = __builtin_amdgcn_raw_buffer_load_b128(r1, id * 16, 1310720, 0);
        const u32x4 p2 = __builtin_amdgcn_raw_buffer_load_b128(r2, id * 16, 0, 0), p3 = __builtin_amdgcn_raw_buffer_load_b128(r3, id * 16, 0, 0);
        bar_lds();
        asm volatile("" :: "v"(p0), "v"(p1), "v"(p2), "v"(p3));
      } else bar_lds();
      ev_wait(L_C + C_F1 * 4, 1);
      weight_share(w, lane, c, h, wqB, w == 3 ? woA : woB, rs, L_U + w * 4096, false);
    }
    STAMP(41);
    ev_wait(L_C + C_X0 * 4, 1);
    STAMP(42);
  }

  for (int layer = 0; layer < 4; ++layer) {
    const int m = layer & 1;
    const unsigned ev = L_C + layer * 256;
    u32x4 q1, k1; u32x2 q2, k2;
    {
      LANE_VARS;
      __builtin_amdgcn_s_setprio(GPRI);
      STAMP(1 + layer * 10);
      const unsigned xoff = OFF_X + layer * XIM2 + (c * 8 + tt) * XT;
      const unsigned xl = lane * 16;
#define XLD(k) ld128(rs, xl + ((k) & 3) * 1024, xoff + ((k) >> 2) * 4096)
#define XISS(ks) (XI0 + 2 * (ks) < XD + (ks) ? XI0 + 2 * (ks) : XD + (ks))
      u32x4 xb[40];
      u32x2 sq[4];
#pragma unroll
      for (int ks = 0; ks < XI0; ++ks) xb[ks] = XLD(ks);
      SB();
      const unsigned wa = opaque(L_A + lane * 16), wb = opaque(L_A + 40960 + lane * 16);
      u32x4 wf[3][2];
      ev_wait(ev + C_WQ * 4, 4);
#pragma unroll
      for (int ks = 0; ks < 2; ++ks) { wf[ks][0] = lds_r128(wa + ks * 2048); wf[ks][1] = lds_r128(wa + ks * 2048 + 1024); }
      f32x16 a0 = zero16, a1 = zero16;
      unsigned seen = 0;
      float rstd = 0.f;
      u32x4 cb, ca0, ca1;
#define SQLD(i) ld64(rs, t * 8 + hh * 1024 + (i) * 256, xoff + 40960)
#define SQMAX umax(umax(umax(sq[0].x, sq[0].y), umax(sq[1].x, sq[1].y)), umax(umax(sq[2].x, sq[2].y), umax(sq[3].x, sq[3].y)))
#pragma unroll
      for (int g = 0; g < 8; ++g) {
        unsigned mx = 0;
#pragma unroll
        for (int i = 0; i < 5; ++i) mx = umax4(mx, xb[5 * g + i]);
        if (g == 7) mx = umax(mx, SQMAX);
        if (__builtin_expect(unwritten(mx), 0)) {
          unsigned spins = 0;
          do {
            if (give_up(spins)) break;
#pragma unroll
            for (int i = 0; i < 5; ++i) xb[5 * g + i] = XLD(5 * g + i);
            mx = 0;
            if (g == 7) {
#pragma unroll
              for (int i = 0; i < 4; ++i) sq[i] = SQLD(i);
              mx = SQMAX;
            }
#pragma unroll
            for (int i = 0; i < 5; ++i) mx = umax4(mx, xb[5 * g + i]);
          } while (unwritten(mx));
#pragma unroll
          for (int i = 5 * g + 5; i < 40; ++i) if (i < XISS(5 * g)) xb[i] = XLD(i);
        }
        if (g == 0) { ev_post(ev + C_INIT * 4); STAMP(10 + layer * 10); }
#pragma unroll
        for (int i = 0; i < 5; ++i) {
          const int ks = 5 * g + i, kn = ks + 2, kx = XISS(ks), ky = XISS(ks + 1) - 1;
          if (kn < 40 && kn % 10 == 8) seen = ev_peek(kn < 38 ? ev + (C_WQ + (kn + 2) / 10) * 4 : L_C);
          if (kn < 40 && kn % 10 == 0) ev_wait_from(seen, ev + (C_WQ + kn / 10) * 4, 4);
          if (kn == 32) seen = ev_peek(L_C + m * 256 + C_CS * 4);
          if (kn == 34) {
            ev_wait_from(seen, L_C + m * 256 + C_CS * 4, 4);
            unsigned h0, m0, l0, h1, m1, l1;
            split3(*(LAS float*)(L_M + m * 256 + t * 4), h0, m0, l0);
            split3(*(LAS float*)(L_M + m * 256 + 128 + t * 4), h1, m1, l1);
            ca0 = (u32x4){(h0 >> 16) | h0, (m0 >> 16) | m0, (h0 >> 16) | l0, 0u};
            ca1 = (u32x4){(h1 >> 16) | h1, (m1 >> 16) | m1, (h1 >> 16) | l1, 0u};
          }
          if (kn < 40) {
            const unsigned ad = kn < 20 ? wa + kn * 2048 : wb + (kn - 20) * 2048;
            wf[kn % 3][0] = lds_r128(ad); wf[kn % 3][1] = lds_r128(ad + 1024);
          }
          SB();
          a0 = mfma32(wf[ks % 3][0], xb[ks], a0);
          if (ky > kx && kx < 40) xb[kx] = XLD(kx);
          if (ks == 36) {
            float sm = (__uint_as_float(sq[0].x) + __uint_as_float(sq[1].x)) + (__uint_as_float(sq[2].x) + __uint_as_float(sq[3].x));
            float ss = (__uint_as_float(sq[0].y) + __uint_as_float(sq[1].y)) + (__uint_as_float(sq[2].y) + __uint_as_float(sq[3].y));
            sm = half_lo(sm) + half_hi(sm);
            ss = half_lo(ss) + half_hi(ss);
            const float mean = sm * (1.f / 640.f);
            rstd = __builtin_amdgcn_rsqf(fmaxf(ss * (1.f / 640.f) - mean * mean, 0.f) + 1e-5f);
            unsigned mh, mm, ml;
            split3(hh ? 0.f : -mean, mh, mm, ml);
            cb = (u32x4){(mh >> 16) | mm, (mh >> 16) | mm, (ml >> 16) | mh, 0u};
          }
          SB();
          a1 = mfma32(wf[ks % 3][1], xb[ks], a1);
          if (ky < 40) xb[ky] = XLD(ky);
          if (ky >= 40 && ky < 44) sq[ky - 40] = SQLD(ky - 40);
          if (ks == 28) ev_post(ev + C_PR * 4);
          SB();
        }
      }
      a1 = mfma32(ca1, cb, a1);
      a0 = mfma32(ca0, cb, a0);
      const unsigned own = m ? y : 4 - y;
      const unsigned tb = L_U + own * UNIT + (tt & 3) * TILE;
      const unsigned vb = tb + T_V + (t >> 3) * VOCT + (t & 7) * 2;
      const unsigned vA = vb + hh * 64, vB = vb + (hh ? 14 : 8) * 16, vC = vb + (hh ? 18 : 12) * 16;
      const float rq = rstd * (0.22360679774997896f * 1.4426950408889634f), r2 = hh ? rstd : rq;
      SB();
      STAMP(2 + layer * 10);
      const unsigned c0 = pack2(a1[8] * rstd, a1[9] * rstd), c1 = pack2(a1[10] * rstd, a1[11] * rstd);
      const unsigned d0 = pack2(a1[12] * rstd, a1[13] * rstd), d1 = pack2(a1[14] * rstd, a1[15] * rstd);
      k1 = (u32x4){pack2(a1[0] * rstd, a1[1] * rstd), pack2(a1[2] * rstd, a1[3] * rstd), pack2(a1[4] * rstd, a1[5] * rstd), pack2(a1[6] * rstd, a1[7] * rstd)};
      k2 = (u32x2){hh ? 0x3F803F80u : c0, hh ? 0x00003F80u : c1};
      lds_w128(tb + lane * 16, k1);
      lds_w64(tb + T_K2 + lane * 8, k2);
      const unsigned cc = hh ? c0 : 0x3F800000u;
      lds_w16lo(vC, cc); lds_w16hi(vC + 16, cc); lds_w16lo(vC + 32, c1); lds_w16hi(vC + 48, c1);
      const unsigned e0 = pack2(a0[8] * r2, a0[9] * r2), e1 = pack2(a0[10] * r2, a0[11] * r2);
      const unsigned f0 = pack2(a0[12] * rstd, a0[13] * rstd), f1 = pack2(a0[14] * rstd, a0[15] * rstd);
      const unsigned b0 = hh ? e0 : d0, b1 = hh ? e1 : d1;
      asm volatile("" ::: "memory");
      lds_w16lo(vB, b0); lds_w16hi(vB + 16, b0); lds_w16lo(vB + 32, b1); lds_w16hi(vB + 48, b1);
      lds_w16lo(vA, f0); lds_w16hi(vA + 16, f0); lds_w16lo(vA + 32, f1); lds_w16hi(vA + 48, f1);
      ev_post(ev + (C_RDY + own) * 4);
      q1 = (u32x4){pack2(a0[0] * rq, a0[1] * rq), pack2(a0[2] * rq, a0[3] * rq), pack2(a0[4] * rq, a0[5] * rq), pack2(a0[6] * rq, a0[7] * rq)};
      q2 = (u32x2){hh ? 0u : e0, hh ? 0u : e1};
      __builtin_amdgcn_s_setprio(0);
    }
    STAMP(3 + layer * 10);

    {
      LANE_VARS;
      const unsigned kvl = OFF_KV + layer * KV_LAYER + h * 16 * UNIT;
      const unsigned vl = T_V + hh * VOCT + vpos(t) * 16;
      const unsigned lk = L_U + lane * 16, lk2 = L_U + T_K2 + lane * 8, lv = L_U + vl;
      f32x16 o = zero16, sa, sb = zero16;
      Frag f;
      f.vb = (u32x4){0u, 0u, 0u, 0u};
      sa = mfma32k8(k2, q2, mfma32(k1, q1, m ? zero16 : tri_mask<true>()));
      float mx = max16(sa);
      mx = fmaxf(half_lo(mx), half_hi(mx));
      set_shift(q2, mx, hh);
      if (m == 1) {
        ev_wait(ev + C_RDY * 4, 4);
        STAMP(4 + layer * 10);
        f.k1 = lds_r128(lk);
        f.k2 = lds_r64(lk2);
        f.va = lds_r128(lv);
        sa = mfma32k8(f.k2, q2, mfma32(f.k1, q1, zero16));
        f.k1 = lds_r128(lk + TILE);
        f.k2 = lds_r64(lk2 + TILE);
        unsigned kb = lk, k2b = lk2, vc = lv, nxt = UNIT;
        if (YPRI == 4) prio_if(y);
        for (int n = 0; n < 16; ++n) {
          if (YPRI >= 1 && YPRI < 4) prio_if(y);
          att_step<1, 1, 1>(sa, sb, o, f, q1, q2, zero16, vc + 2 * VOCT, kb + 2 * TILE, k2b + 2 * TILE, vc + TILE);
          if (YPRI == 1) __builtin_amdgcn_s_setprio(0);
          const unsigned seen = ev_peek(ev + (C_RDY + n + 1) * 4);
          att_step<1, 1, 1>(sb, sa, o, f, q1, q2, zero16, vc + TILE + 2 * VOCT, kb + 3 * TILE, k2b + 3 * TILE, vc + 2 * TILE);
          ev_wait_from<ONAP>(seen, ev + (C_RDY + n + 1) * 4, n < 15 ? 4 : 0);
          kb = lk + nxt; k2b = lk2 + nxt;
          const unsigned vn = lv + nxt;
          if (YPRI == 2) __builtin_amdgcn_s_setprio(0);
          att_step<1, 1, 1>(sa, sb, o, f, q1, q2, zero16, vc + 2 * TILE + 2 * VOCT, kb, k2b, vc + 3 * TILE);
          if (YPRI == 3) __builtin_amdgcn_s_setprio(0);
          att_step<1, 1, 1>(sb, sa, o, f, q1, q2, zero16, vc + 3 * TILE + 2 * VOCT, kb + TILE, k2b + TILE, vn);
          ev_post(ev + (C_DONE + n) * 4);
          vc = vn;
          nxt = nxt == 4 * UNIT ? 0 : nxt + UNIT;
        }
        o = pv_hi(f.vb, sb, o);
      } else {
#pragma unroll
        for (int i = 0; i < 16; ++i) sa[i] -= mx;
        const f32x16 far = tri_mask<false>();
        const unsigned ka = lk + (4 + tt) * TILE, k2a = lk2 + (4 + tt) * TILE, va = lv + (4 + tt) * TILE;
        f.va = lds_r128(va + 8 * TILE);
        SB();
        ev_wait(ev + (C_RDY + 4 - y) * 4, 4);
        const int x1 = tt & 3;
        const unsigned e1 = ev + (C_RDY + 3 - y) * 4, e2 = e1 - 4;
        ev_wait_if_eq(x1, 0, e1, 4);
        STAMP(4 + layer * 10);
        f.k1 = lds_r128(ka + 7 * TILE);
        f.k2 = lds_r64(k2a + 7 * TILE);
#define STEP_W(n, A, B) ev_wait_if_eq(x1, ((n) + 1) & 3, (n) < 3 ? e1 : e2, 4); \
        att_step<1, 1, 1>(A, B, o, f, q1, q2, zero16, va + (8 - (n)) * TILE + 2 * VOCT, ka + (6 - (n)) * TILE, k2a + (6 - (n)) * TILE, va + (7 - (n)) * TILE)
        STEP_W(0, sa, sb);
        STEP_W(1, sb, sa);
        STEP_W(2, sa, sb);
        STEP_W(3, sb, sa);
        STEP_W(4, sa, sb);
        STEP_W(5, sb, sa);
        STEP_W(6, sa, sb);
        att_step<1, 0, 1>(sb, sa, o, f, q1, q2, far, va + TILE + 2 * VOCT, 0, 0, va);
        att_step<0, 0, 0>(sa, sb, o, f, q1, q2, zero16, va + 2 * VOCT, 0, 0, 0);
        o = pv_hi(f.vb, sa, o);
      }
      __builtin_amdgcn_s_setprio(2);
      STAMP(5 + layer * 10);
      const float tot = ((o[0] + o[1]) + (o[2] + o[3])) + ((o[4] + o[5]) + (o[6] + o[7])) + ((o[8] + o[9]) + (o[10] + o[11]));
      const bool bad = !(fabsf(tot) <= 3.0e38f) | !(o[8] <= (hh ? SUM_LIMIT : 3.0e38f));
      if (__builtin_expect(__builtin_amdgcn_ballot_w64(bad) != 0, 0)) {
        const int qt = c * 8 + tt;
        const int lo = m ? 0 : (qt >= 8 ? qt - 8 : 0), hi = m ? 63 : qt;
        ev_wait(ev + C_PUB * 4, 4);
        if (hh) { q2.x = 0u; q2.y = 0u; }
        mx = -3.0e38f;
        for (int pass = 0; pass < 2; ++pass) {
          o = zero16;
          for (int kt = lo; kt <= hi; ++kt) {
            const unsigned so = kvl + kt * TILE;
            const u32x4 j1 = ld128(rs, lane * 16, so), v0 = ld128(rs, vl, so), v1 = ld128(rs, vl + 2 * VOCT, so);
            const u32x2 j2 = ld64(rs, lane * 8 + T_K2, so);
            f32x16 cin = zero16;
            if (!m && kt == qt) cin = tri_mask<true>();
            if (!m && kt == qt - 8) cin = tri_mask<false>();
            f32x16 s = mfma32k8(j2, q2, mfma32(j1, q1, cin));
            if (pass == 0) {
              mx = fmaxf(mx, max16(s));
            } else {
#pragma unroll
              for (int e = 0; e < 16; ++e) s[e] = __builtin_amdgcn_exp2f(s[e]);
              o = pv_lo(v0, s, o);
              o = pv_hi(v1, s, o);
            }
          }
          if (pass == 0) { mx = fmaxf(half_lo(mx), half_hi(mx)); set_shift(q2, mx, hh); }
        }
      }
      const float inv = __builtin_amdgcn_rcpf(half_hi(o[8]));
      const u32x4 p = {pack2(o[0] * inv, o[1] * inv), pack2(o[2] * inv, o[3] * inv), pack2(o[4] * inv, o[5] * inv), pack2(o[6] * inv, o[7] * inv)};
      const unsigned oo = OFF_O + layer * XIMG + (c * 16 + tt * 2) * 20480;
      const unsigned lo = (t >> 4) * 20480 + (t & 15) * 16;
      st128(clean4(p), rs, lo + hh * 256, oo + h * 512);
      st64(clean2((u32x2){pack2(o[8] * inv, o[9] * inv), pack2(o[10] * inv, o[11] * inv)}), rs, hh ? OOB : lo, oo + (64 + (h >> 1)) * 256 + (h & 1) * 8);
    }
    STAMP(6 + layer * 10);

    {
      LANE_VARS;
      const int cg = h & 7, t16 = lane & 15, g4 = lane >> 4, kh = (w >> 1) & 1;
      const int rt = c * 16 + ((h >> 3) + 4 - 4 * y) * 2 + (w & 1);
      const int row = rt * 16 + t16;
      const unsigned xl = (row >> 5) * XT + (row & 31) * 16 + cg * 5120;
      const unsigned xo = OFF_X + layer * XIM2;
      const unsigned xr = kh ? OOB : xo;
      const u32x4 ra = ld128(rs, xl + g4 * 512, xr), rb = ld128(rs, xl + (4 + g4) * 512, xr);
      const u32x2 rc = ld64(rs, xl + (8 + (g4 >> 1)) * 512 + (g4 & 1) * 8, xr);
      const unsigned oo = OFF_O + layer * XIMG + rt * 20480 + kh * 10240;
      const unsigned ol = lane * 16;
#define OLD(k) ld128(rs, ol + ((k) & 3) * 1024, oo + ((k) >> 2) * 4096)
      u32x4 ob[10];
      const unsigned wa0 = opaque(L_A + kh * 51200 + lane * 16);
      constexpr int WD = OWD;
      u32x4 wf[WD + 1];
      if (ODRAIN) drain_vm();
      ev_post(ev + C_OS * 4);
      f32x4 acc[5];
      acc[0] = (f32x4){bf_lo(ra.x), bf_hi(ra.x), bf_lo(ra.y), bf_hi(ra.y)};
      acc[1] = (f32x4){bf_lo(ra.z), bf_hi(ra.z), bf_lo(ra.w), bf_hi(ra.w)};
      acc[2] = (f32x4){bf_lo(rb.x), bf_hi(rb.x), bf_lo(rb.y), bf_hi(rb.y)};
      acc[3] = (f32x4){bf_lo(rb.z), bf_hi(rb.z), bf_lo(rb.w), bf_hi(rb.w)};
      acc[4] = (f32x4){bf_lo(rc.x), bf_hi(rc.x), bf_lo(rc.y), bf_hi(rc.y)};
      ev_wait(ev + C_OS * 4, OSN);
      if (OSLP) __builtin_amdgcn_s_sleep(OSLP);
#pragma unroll
      for (int ks = 0; ks < 10; ++ks) ob[ks] = OLD(ks);
      SB();
      ev_wait(ev + C_WO * 4, 4);
#pragma unroll
      for (int i = 0; i < WD; ++i) wf[i] = lds_r128(wa0 + i * 1024);
#pragma unroll
      for (int g = 0; g < 2; ++g) {
        unsigned mx = 0;
#pragma unroll
        for (int i = 0; i < 5; ++i) mx = umax4(mx, ob[5 * g + i]);
        if (__builtin_expect(unwritten(mx), 0)) {
          unsigned spins = 0;
          do {
            if (give_up(spins)) break;
#pragma unroll
            for (int i = 5 * g; i < 10; ++i) ob[i] = OLD(i);
            mx = 0;
#pragma unroll
            for (int i = 0; i < 5; ++i) mx = umax4(mx, ob[5 * g + i]);
          } while (unwritten(mx));
        }
        if (g == 0) { ev_post(ev + C_OL * 4); STAMP(7 + layer * 10); }
#pragma unroll
        for (int e = 0; e < 25; ++e) {
          const int i = 25 * g + e, in = i + WD;
          if (in < 50) wf[in % (WD + 1)] = lds_r128(wa0 + in * 1024);
          acc[i % 5] = mfma16(wf[i % (WD + 1)], ob[i / 5], acc[i % 5]);
          SB();
        }
      }
      STAMP(8 + layer * 10);
      const unsigned sc = L_U + (y * 2 + (w & 1)) * 5120 + lane * 16;
      const unsigned pe = ev + (y * 2 + (w & 1)) * 4;
      if (kh) {
#pragma unroll
        for (int n = 0; n < 5; ++n) *(LAS f32x4*)(sc + n * 1024) = acc[n];
        ev_post(pe + C_RED * 4);
      } else {
        ev_wait<0>(pe + C_RED * 4, 1);
#pragma unroll
        for (int n = 0; n < 5; ++n) acc[n] += *(LAS f32x4*)(sc + n * 1024);
        const u32x4 na = {pack2(acc[0][0], acc[0][1]), pack2(acc[0][2], acc[0][3]), pack2(acc[1][0], acc[1][1]), pack2(acc[1][2], acc[1][3])};
        const u32x4 nb = {pack2(acc[2][0], acc[2][1]), pack2(acc[2][2], acc[2][3]), pack2(acc[3][0], acc[3][1]), pack2(acc[3][2], acc[3][3])};
        const u32x2 nc = {pack2(acc[4][0], acc[4][1]), pack2(acc[4][2], acc[4][3])};
        if (layer < 3) {
          st128(clean4(na), rs, xl + g4 * 512, xo + XIM2);
          st128(clean4(nb), rs, xl + (4 + g4) * 512, xo + XIM2);
          st64(clean2(nc), rs, xl + (8 + (g4 >> 1)) * 512 + (g4 & 1) * 8, xo + XIM2);
          ev_post(ev + C_OP * 4);
          float s0 = sq2(na.x, 0.f), s1 = sum2(na.x, 0.f);
          s0 = sq2(na.y, s0); s1 = sum2(na.y, s1); s0 = sq2(na.z, s0); s1 = sum2(na.z, s1); s0 = sq2(na.w, s0); s1 = sum2(na.w, s1);
          s0 = sq2(nb.x, s0); s1 = sum2(nb.x, s1); s0 = sq2(nb.y, s0); s1 = sum2(nb.y, s1); s0 = sq2(nb.z, s0); s1 = sum2(nb.z, s1);
          s0 = sq2(nb.w, s0); s1 = sum2(nb.w, s1); s0 = sq2(nc.x, s0); s1 = sum2(nc.x, s1); s0 = sq2(nc.y, s0); s1 = sum2(nc.y, s1);
          s0 = half_lo(s0) + half_hi(s0);
          s1 = half_lo(s1) + half_hi(s1);
          const auto q0 = __builtin_amdgcn_permlane16_swap(__float_as_uint(s0), __float_as_uint(s0), false, false);
          const auto q1 = __builtin_amdgcn_permlane16_swap(__float_as_uint(s1), __float_as_uint(s1), false, false);
          s0 = __uint_as_float(q0[0]) + __uint_as_float(q0[1]);
          s1 = __uint_as_float(q1[0]) + __uint_as_float(q1[1]);
          st64(clean2((u32x2){__float_as_uint(s1), __float_as_uint(s0)}), rs, lane < 16 ? (row >> 5) * XT + 40960 + (row & 31) * 8 + cg * 256 : OOB, xo + XIM2);
          if (XDRAIN) asm volatile("s_waitcnt vmcnt(1)" ::: "memory");
          ev_post(ev + C_XS * 4);
        } else {
          const unsigned od = row * 1280 + cg * 160;
          st128a<0>(na, ro, od + g4 * 16, 0);
          st128a<0>(nb, ro, od + 64 + g4 * 16, 0);
          __builtin_amdgcn_raw_buffer_store_b64(nc, ro, od + 128 + g4 * 8, 0, 0);
        }
      }
      if (layer < 3) { ev_wait(ev + C_XS * 4, 4); if (XSLP) __builtin_amdgcn_s_sleep(XSLP); }
    }
    STAMP(9 + layer * 10);
  }
  STAMP_OUT();
}

extern "C" void kernel_launch(void* const* d_in, const int* in_sizes, int n_in, void* d_out, int out_size, void* d_ws, size_t ws_size,
                              hipStream_t stream) {
  (void)in_sizes; (void)n_in; (void)out_size; (void)ws_size;
  (void)hipFuncSetAttribute((const void*)main_kernel, hipFuncAttributeMaxDynamicSharedMemorySize, LDS_BYTES);
  main_kernel<<<256, 768, LDS_BYTES, stream>>>((const u16*)d_in[0], (const u16*)d_in[1], (const u16*)d_in[2], (const u16*)d_in[4],
                                               (const u16*)d_in[5], (char*)d_ws, (u16*)d_out);
}
```

```cpp
#include <hip/hip_runtime.h>
#include <hip/hip_bf16.h>

typedef float f32x16 __attribute__((ext_vector_type(16)));
typedef float f32x8 __attribute__((ext_vector_type(8)));
typedef float f32x4 __attribute__((ext_vector_type(4)));
typedef unsigned u32x4 __attribute__((ext_vector_type(4)));
typedef unsigned u32x2 __attribute__((ext_vector_type(2)));
typedef __bf16 bf16x16 __attribute__((ext_vector_type(16)));
typedef __bf16 bf16x8 __attribute__((ext_vector_type(8)));
typedef __bf16 bf16x2 __attribute__((ext_vector_type(2)));
typedef short s16x4 __attribute__((ext_vector_type(4)));
typedef unsigned short u16;

#define LAS __attribute__((address_space(3)))
#define GAS __attribute__((address_space(1)))
typedef unsigned long long u64;
typedef GAS u64 gu64;
#define RLX_AGENT __ATOMIC_RELAXED, __HIP_MEMORY_SCOPE_AGENT
#define DEVI __device__ __forceinline__

constexpr unsigned WQ_HEAD = 81920;
constexpr unsigned WO_CG = 102400;
constexpr unsigned XIMG = 2621440;
constexpr unsigned XT = 45056, XIM2 = 64 * XT;
constexpr unsigned VOCT = 352;
constexpr unsigned TILE = 2944, T_K2 = 1024, T_V = 1536;
constexpr unsigned UNIT = 4 * TILE;
constexpr unsigned KV_LAYER = 32 * 64 * TILE;
constexpr unsigned OFF_FLAG = 0;
constexpr unsigned OFF_F1 = 4096, OFF_F2 = OFF_F1 + 32768;
constexpr unsigned OFF_KF = OFF_F2 + 32768;
constexpr unsigned OFF_WQ = OFF_KF + 4 * 32 * 16 * 128;
constexpr unsigned OFF_WO = OFF_WQ + 2 * 32 * WQ_HEAD;
constexpr unsigned OFF_X = OFF_WO + 2 * 8 * WO_CG;
constexpr unsigned OFF_O = OFF_X + 4 * XIM2;
constexpr unsigned OFF_KV = OFF_O + 4 * XIMG;
constexpr unsigned WS_USED = OFF_KV + 4 * KV_LAYER;
constexpr unsigned OFF_DBG = WS_USED;
constexpr unsigned SENT = 0xFFFFFFFFu;
constexpr unsigned OOB = 0xE0000000u;

constexpr unsigned L_A = 0;
constexpr unsigned L_U = 102400;
constexpr unsigned L_M = L_U + 5 * UNIT;
constexpr unsigned L_C = L_M + 512;
constexpr unsigned LDS_BYTES = 163840;
enum { C_RDY = 0, C_DONE = 16, C_WO = 32, C_OP = 33, C_WQ = 34, C_RED = 38, C_XS = 42, C_GO = 46, C_PR = 47, C_W0 = 48, C_X0 = 49, C_INIT = 50, C_SEEN = 51, C_CS = 52, C_PUB = 53, C_F1 = 54, C_OL = 55, C_OS = 56, C_XG = 58 };

constexpr unsigned kSpinCap = 1u << 15;
#define NODOT 0
#define ABL 0
#define XPG 2
#define OWD 16
#define HPRIO 1
#define WIN 10
#define TOUCH 0
#define YPRI 2
#define ONAP 2
#define HNAP 2
#define GPRI 1
#define OLN 4
#define PREF 1
#define RWIN 3
#define XI0 8
#define XD 12
#define OSN 8
#define ODRAIN 0
#define OSLP 6
#define XSLP 4
#define MIX 1
#define LAG 0
#define XEARLY 1
#define STREAM 0
#define RPOS 1
#define RSLP 2
#define X0SLP 10
#define LOE 0
#define OLI 0
#define LNAP 8
#define WOE 40
#define GOAT 36
#define XDRAIN 0
#define PACE 3
#define XPD 16
#define MAPC 2
#define SUM_LIMIT 1.0e18f

DEVI unsigned pack2(float a, float b) {
  bf16x2 v = {(__bf16)a, (__bf16)b};
  return __builtin_bit_cast(unsigned, v);
}
DEVI float sq2(unsigned u, float acc) {
  const bf16x2 p = __builtin_bit_cast(bf16x2, u);
  return __builtin_amdgcn_fdot2_f32_bf16(p, p, acc, false);
}
DEVI float bf_lo(unsigned u) { return __uint_as_float(u << 16); }
DEVI float bf_hi(unsigned u) { return __uint_as_float(u & 0xffff0000u); }
DEVI f32x16 mfma32(u32x4 a, u32x4 b, f32x16 c) {
  return __builtin_amdgcn_mfma_f32_32x32x16_bf16(__builtin_bit_cast(bf16x8, a), __builtin_bit_cast(bf16x8, b), c, 0, 0, 0);
}
DEVI f32x16 mfma32k8(u32x2 a, u32x2 b, f32x16 c) {
  return __builtin_amdgcn_mfma_f32_32x32x8bf16_1k(__builtin_bit_cast(s16x4, a), __builtin_bit_cast(s16x4, b), c, 0, 0, 0);
}
DEVI f32x4 mfma16(u32x4 a, u32x4 b, f32x4 c) {
  return __builtin_amdgcn_mfma_f32_16x16x32_bf16(__builtin_bit_cast(bf16x8, a), __builtin_bit_cast(bf16x8, b), c, 0, 0, 0);
}
DEVI f32x16 pv_lo(u32x4 v, const f32x16& p, f32x16 o) {
  f32x8 h = __builtin_shufflevector(p, p, 0, 1, 2, 3, 4, 5, 6, 7);
  return __builtin_amdgcn_smfmac_f32_32x32x32_bf16(__builtin_bit_cast(bf16x8, v), __builtin_bit_cast(bf16x16, h), o, 0xDDDD, 0, 0);
}
DEVI f32x16 pv_hi(u32x4 v, const f32x16& p, f32x16 o) {
  f32x8 h = __builtin_shufflevector(p, p, 8, 9, 10, 11, 12, 13, 14, 15);
  return __builtin_amdgcn_smfmac_f32_32x32x32_bf16(__builtin_bit_cast(bf16x8, v), __builtin_bit_cast(bf16x16, h), o, 0xDDDD, 0, 0);
}
DEVI u32x4 lds_r128(unsigned a) { return *(LAS u32x4*)a; }
DEVI u32x2 lds_r64(unsigned a) { return *(LAS u32x2*)a; }
DEVI void lds_w128(unsigned a, u32x4 v) { *(LAS u32x4*)a = v; }
DEVI void lds_w64(unsigned a, u32x2 v) { *(LAS u32x2*)a = v; }
DEVI void lds_w16(unsigned a, float v) { *(LAS __bf16*)a = (__bf16)v; }
DEVI float half_lo(float v) { return __uint_as_float(__builtin_amdgcn_permlane32_swap(__float_as_uint(v), __float_as_uint(v), false, false)[0]); }
DEVI float half_hi(float v) { return __uint_as_float(__builtin_amdgcn_permlane32_swap(__float_as_uint(v), __float_as_uint(v), false, false)[1]); }
DEVI unsigned umax(unsigned a, unsigned b) { return a > b ? a : b; }
DEVI unsigned umax4(unsigned m, u32x4 v) { return umax(umax(umax(umax(m, v.x), v.y), v.z), v.w); }
DEVI bool unwritten(unsigned m) { return __builtin_amdgcn_ballot_w64(m == SENT) != 0; }
DEVI unsigned clean(unsigned v) { return v < SENT ? v : SENT - 1; }
DEVI u32x4 clean4(u32x4 v) { return (u32x4){clean(v.x), clean(v.y), clean(v.z), clean(v.w)}; }
DEVI u32x2 clean2(u32x2 v) { return (u32x2){clean(v.x), clean(v.y)}; }
DEVI unsigned vpos(int d) {
  const unsigned q = ((14u | 0u << 5 | 4u << 10 | 18u << 15 | 8u << 20) >> ((d >> 2) * 5 & 31)) & 31u;
  return d < 20 ? q + (d & 3) : (d == 20 ? 13u : 12u);
}
DEVI void lds_w16lo(unsigned a, unsigned v) { *(LAS u16*)a = (u16)v; }
DEVI void lds_w16hi(unsigned a, unsigned v) { *(LAS u16*)a = (u16)(v >> 16); }
DEVI float sum2(unsigned u, float acc) {
  const bf16x2 ones = {(__bf16)1.0f, (__bf16)1.0f};
  return __builtin_amdgcn_fdot2_f32_bf16(__builtin_bit_cast(bf16x2, u), ones, acc, false);
}
DEVI void split3(float v, unsigned& hi, unsigned& mid, unsigned& lo) {
  hi = __float_as_uint(v) & 0xffff0000u;
  const float r1 = v - __uint_as_float(hi);
  mid = __float_as_uint(r1) & 0xffff0000u;
  lo = __float_as_uint(r1 - __uint_as_float(mid)) & 0xffff0000u;
}
#define SB() __builtin_amdgcn_sched_barrier(0)
DEVI void bar_lds() {
  asm volatile("s_waitcnt lgkmcnt(0)\n\ts_barrier" ::: "memory");
}
DEVI void drain_vm() { asm volatile("s_waitcnt vmcnt(0)" ::: "memory"); }
#define DMA_ASM(d, v, r, o) "s_mov_b32 m0, " d "\n\ts_nop 3\n\tbuffer_load_dwordx4 " v ", " r ", " o " offen sc1 lds\n\t"
DEVI void dma1k(unsigned lds_dst, unsigned voff, u32x4 srd, unsigned soff) {
  unsigned keep;
  asm volatile("s_mov_b32 %0, m0\n\t" DMA_ASM("%1", "%2", "%3", "%4") "s_mov_b32 m0, %0"
               : "=&s"(keep) : "s"(__builtin_amdgcn_readfirstlane(lds_dst)), "v"(voff), "s"(srd), "s"(__builtin_amdgcn_readfirstlane(soff)) : "memory");
}
constexpr unsigned NO_LDS = 0x30000;
typedef __amdgpu_buffer_rsrc_t rsrc_t;
DEVI u32x4 ld128(rsrc_t r, unsigned voff, unsigned soff) { return __builtin_amdgcn_raw_buffer_load_b128(r, voff, soff, 16); }
DEVI u32x2 ld64(rsrc_t r, unsigned voff, unsigned soff) { return __builtin_amdgcn_raw_buffer_load_b64(r, voff, soff, 16); }
template <int AUX> DEVI void st128a(u32x4 v, rsrc_t r, unsigned voff, unsigned soff) {
  __builtin_amdgcn_raw_buffer_store_b128(v, r, voff, soff, AUX);
  asm volatile("s_nop 1" :: "v"(v) : "memory");
}
DEVI void st128(u32x4 v, rsrc_t r, unsigned voff, unsigned soff) { st128a<16>(v, r, voff, soff); }
DEVI void st64(u32x2 v, rsrc_t r, unsigned voff, unsigned soff) { __builtin_amdgcn_raw_buffer_store_b64(v, r, voff, soff, 16); }

DEVI bool give_up(unsigned& spins) {
  __builtin_amdgcn_s_sleep(2);
  return ++spins > kSpinCap;
}
DEVI u32x2 next_tag(u32x2 g) {
  u64 v = ((u64)g.y << 32 | g.x) + 1;
  v += v == ~0ull;
  return (u32x2){(unsigned)v, (unsigned)(v >> 32)};
}
DEVI void wait_tag(rsrc_t rs, unsigned off, u32x2 tag) {
  for (unsigned spins = 0;;) {
    const u32x2 v = ld64(rs, off, 0);
    if (__builtin_amdgcn_ballot_w64(off != OOB && (v.x != tag.x || v.y != tag.y)) == 0 || give_up(spins)) break;
  }
  asm volatile("" ::: "memory");
}

#define STAMP(i) do {} while (0)
#define STAMP_OUT() do {} while (0)

DEVI void transpose8x4(const u32x2 (&r)[8], u32x4 (&o)[4]) {
#pragma unroll
  for (int p = 0; p < 4; ++p) {
    o[0][p] = __builtin_amdgcn_perm(r[2 * p + 1].x, r[2 * p].x, 0x05040100u);
    o[1][p] = __builtin_amdgcn_perm(r[2 * p + 1].x, r[2 * p].x, 0x07060302u);
    o[2][p] = __builtin_amdgcn_perm(r[2 * p + 1].y, r[2 * p].y, 0x05040100u);
    o[3][p] = __builtin_amdgcn_perm(r[2 * p + 1].y, r[2 * p].y, 0x07060302u);
  }
}

DEVI void wq_item_load(u32x2 (&r)[8], rsrc_t rw, int h, int kh, int qd, bool on) {
  const int mt = qd >> 3, rq = qd & 7;
  const int col = h * 20 + (rq < 5 ? mt * 640 + 4 * rq : 1280 + mt * 12 + 4 * (rq - 5));
  const unsigned off = (on && qd < 15) ? kh * 8 * 3840 + col * 2 : OOB;
#pragma unroll
  for (int j = 0; j < 8; ++j) r[j] = __builtin_amdgcn_raw_buffer_load_b64(rw, off, j * 3840, 0);
}
DEVI unsigned wq_item_pos(int kh, int qd) { return (kh >> 1) * 2048 + (qd >> 3) * 1024 + (kh & 1) * 512 + (qd & 7) * 64; }

struct Frag { u32x4 k1; u32x2 k2; u32x4 va, vb; };

template <bool QK, bool LDK, bool LDV>
DEVI void att_step(f32x16& cur, f32x16& oth, f32x16& o, Frag& f, const u32x4& q1, const u32x2& q2, const f32x16& cin,
                   unsigned a_vb, unsigned a_k1, unsigned a_k2, unsigned a_va) {
  o = pv_hi(f.vb, oth, o);
  f.vb = lds_r128(a_vb);
  SB();
#pragma unroll
  for (int i = 0; i < 4; ++i) cur[i] = __builtin_amdgcn_exp2f(cur[i]);
  SB();
  if (QK) oth = mfma32(f.k1, q1, cin);
  if (LDK) f.k1 = lds_r128(a_k1);
  SB();
#pragma unroll
  for (int i = 4; i < 8; ++i) cur[i] = __builtin_amdgcn_exp2f(cur[i]);
  SB();
  if (QK) oth = mfma32k8(f.k2, q2, oth);
  if (LDK) f.k2 = lds_r64(a_k2);
  SB();
#pragma unroll
  for (int i = 8; i < 12; ++i) cur[i] = __builtin_amdgcn_exp2f(cur[i]);
  SB();
  o = pv_lo(f.va, cur, o);
  if (LDV) f.va = lds_r128(a_va);
  SB();
#pragma unroll
  for (int i = 12; i < 16; ++i) cur[i] = __builtin_amdgcn_exp2f(cur[i]);
  SB();
}
DEVI void bar_if_eq(int a, int b) {
  asm volatile("s_cmp_lg_u32 %0, %1\n\ts_cbranch_scc1 .Lskip%=\n\ts_waitcnt lgkmcnt(0)\n\ts_barrier\n.Lskip%=:" :: "s"(a), "s"(b) : "memory", "scc");
}
DEVI void bar_if_ne(int a, int b) {
  asm volatile("s_cmp_eq_u32 %0, %1\n\ts_cbranch_scc1 .Lskip%=\n\ts_waitcnt lgkmcnt(0)\n\ts_barrier\n.Lskip%=:" :: "s"(a), "s"(b) : "memory", "scc");
}
DEVI void prio_if(int on) {
  asm volatile("s_cmp_eq_u32 %0, 0\n\ts_cbranch_scc1 .Lskip%=\n\ts_setprio 1\n.Lskip%=:" :: "s"(on) : "scc");
}
DEVI int opaque(int v) { asm volatile("" : "+v"(v)); return v; }

DEVI float max16(const f32x16& s) {
  float m = fmaxf(fmaxf(s[0], s[1]), s[2]);
#pragma unroll
  for (int i = 3; i < 15; i += 2) m = fmaxf(fmaxf(m, s[i]), s[i + 1]);
  return fmaxf(m, s[15]);
}
DEVI void set_shift(u32x2& q2, float m, int hh) {
  unsigned hi, mid, lo;
  split3(-m, hi, mid, lo);
  q2.x = hh ? (hi >> 16) | mid : q2.x;
  q2.y = hh ? (lo >> 16) | 0xFF7F0000u : q2.y;
}
template <bool DIAG> DEVI f32x16 tri_mask() {
  f32x16 c;
#pragma unroll
  for (int i = 0; i < 16; ++i) {
    const int key = (i & 3) + 8 * (i >> 2);
    const u64 above = ((1ull << key) - 1) | ((1ull << (key + 4)) - 1) << 32;
    c[i] = __builtin_amdgcn_inverse_ballot_w64(DIAG ? above : ~above) ? -3.0e38f : 0.f;
  }
  return c;
}

DEVI void ev_wait_if_eq(int a, int b, unsigned addr, unsigned target) {
  unsigned sv, cnt, v;
  asm volatile("s_cmp_lg_u32 %3, %4\n\ts_cbranch_scc1 .Lgo%=\n\ts_mov_b32 %1, 0x100000\n\ts_nop 7\n\ts_nop 3\n"
               ".Lspin%=:\n\tds_read_b32 %2, %5\n\ts_waitcnt lgkmcnt(0)\n\tv_readfirstlane_b32 %0, %2\n\t"
               "s_cmp_ge_u32 %0, %6\n\ts_cbranch_scc1 .Lgo%=\n\ts_sleep 1\n\ts_sub_u32 %1, %1, 1\n\ts_cbranch_scc0 .Lspin%=\n"
               ".Lgo%=:"
               : "=&s"(sv), "=&s"(cnt), "=&v"(v) : "s"(a), "s"(b), "v"(addr), "s"(target) : "memory", "scc");
}
DEVI void ev_post(unsigned addr, unsigned n = 1) {
  asm volatile("s_mov_b64 exec, 1\n\tds_add_u32 %0, %1\n\ts_mov_b64 exec, -1" :: "v"(addr), "v"(n) : "memory");
}
DEVI unsigned ev_peek(unsigned addr) { return *(volatile LAS unsigned*)addr; }
template <int NAP = 1> DEVI void ev_wait_from(unsigned seen, unsigned addr, unsigned target) {
  unsigned sv, cnt;
  asm volatile("s_nop 0\n\tv_readfirstlane_b32 %0, %2\n\ts_cmp_ge_u32 %0, %4\n\ts_cbranch_scc1 .Lgo%=\n\t"
               "s_mov_b32 %1, 0x100000\n"
               ".Lspin%=:\n\ts_sleep %5\n\tds_read_b32 %2, %3\n\ts_sub_u32 %1, %1, 1\n\ts_cbranch_scc1 .Lout%=\n\t"
               "s_waitcnt lgkmcnt(0)\n\tv_readfirstlane_b32 %0, %2\n\t"
               "s_cmp_ge_u32 %0, %4\n\ts_cbranch_scc0 .Lspin%=\n"
               ".Lout%=:\n\ts_waitcnt lgkmcnt(0)\n"
               ".Lgo%=:"
               : "=&s"(sv), "=&s"(cnt), "+&v"(seen)
               : "v"(addr), "s"(__builtin_amdgcn_readfirstlane(target)), "i"(NAP) : "memory", "scc");
}
template <int NAP = 1> DEVI void ev_wait(unsigned addr, unsigned target) { ev_wait_from<NAP>(ev_peek(addr), addr, target); }

DEVI void kv_publish(unsigned l0, unsigned l5, rsrc_t rs, unsigned un, unsigned slot) {
  u32x4 g[6];
#pragma unroll
  for (int i = 0; i < 6; ++i) g[i] = lds_r128(L_U + slot * UNIT + l0 + i * 2048);
#pragma unroll
  for (int i = 0; i < 6; ++i) st128(g[i], rs, i < 5 ? l0 : l5, OFF_KV + un * UNIT + i * 2048);
}
DEVI void kv_fetch(unsigned l16, u32x4 srd, unsigned un, unsigned slot, int first, int step) {
  for (int i = first; i < 12; i += step) {
    const unsigned o = i < 11 ? i * 1024 : UNIT - 1024;
    dma1k(L_U + slot * UNIT + o, l16, srd, OFF_KV + un * UNIT + o);
  }
}
DEVI int kv_order(int c, int j) { return c < 4 ? (j < 4 ? (c - j) & 3 : 4 + ((c - j) & 3)) : (j < 5 ? (c - j) & 3 : 4 + ((c - j) & 3)); }
DEVI void kv_fake(unsigned l0, unsigned l5, unsigned slot) {
#pragma unroll
  for (int i = 0; i < 6; ++i) {
    const unsigned o = (l0 + i * 2048) % TILE, one = (o >= 1280 && o < T_V) ? 0x3F800000u : 0u;
    if (i < 5 || l5 != OOB) lds_w128(L_U + slot * UNIT + l0 + i * 2048, (u32x4){0u, one, 0u, one});
  }
}
DEVI unsigned o_piece(unsigned q, int l, int c, int h) { return OFF_O + l * XIMG + c * 327680 + h * 10240 + q * 1024; }
DEVI unsigned x_piece(unsigned q, int l, int c, int h) { return OFF_X + l * XIM2 + c * 8 * XT + h * (XT / 4) + q * 1024; }
DEVI void add_colsum(const u32x4 (&wn)[20], int u, int lane, unsigned dst) {
  float s0 = 0.f, s1 = 0.f;
  const bf16x2 ones = {(__bf16)1.0f, (__bf16)1.0f};
#pragma unroll
  for (int i = 0; i < 20; ++i) {
    const unsigned a = wn[i].x, b = wn[i].y, c = wn[i].z, d = wn[i].w;
    s0 = __builtin_amdgcn_fdot2_f32_bf16(__builtin_bit_cast(bf16x2, a), ones, s0, false);
    s1 = __builtin_amdgcn_fdot2_f32_bf16(__builtin_bit_cast(bf16x2, b), ones, s1, false);
    s0 = __builtin_amdgcn_fdot2_f32_bf16(__builtin_bit_cast(bf16x2, c), ones, s0, false);
    s1 = __builtin_amdgcn_fdot2_f32_bf16(__builtin_bit_cast(bf16x2, d), ones, s1, false);
  }
  s0 += s1;
  s0 = half_lo(s0) + half_hi(s0);
  if (lane < 32) __hip_atomic_fetch_add((LAS float*)(dst + (u & 1) * 128 + lane * 4), s0, __ATOMIC_RELAXED, __HIP_MEMORY_SCOPE_WORKGROUP);
}

DEVI void weight_share(int task, int lane, int c, int h, const u16* wq, const u16* wo, rsrc_t rs, unsigned bounce, bool bar) {
  const int m2 = task >> 2, wi = task & 3;
  u32x2 r[8];
  u32x4 o[4];
  unsigned dst, pos, np;
  if (wi < 3) {
    const rsrc_t rw = __builtin_amdgcn_make_buffer_rsrc((void*)wq, 0, 640 * 3840, 0x00020000);
    wq_item_load(r, rw, h, c * 10 + wi * 4 + (lane >> 4), lane & 15, wi < 2 || lane < 32);
    pos = wq_item_pos(lane >> 4, lane & 15);
    dst = OFF_WQ + (m2 * 32 + h) * WQ_HEAD + (c * 10 + wi * 4) * 1024;
    np = wi < 2 ? 4 : 2;
  } else {
    const int b32 = (h >> 3) * 8 + c, p = b32 * 4 + (lane >> 4), nt = p % 5, q = (p / 5) * 4 + ((lane >> 2) & 3), g = lane & 3;
    const int c4 = nt < 4 ? (nt >> 1) * 8 + g * 2 + (nt & 1) : 16 + g;
    const rsrc_t rw = __builtin_amdgcn_make_buffer_rsrc((void*)wo, 0, 640 * 1280, 0x00020000);
    const int k0 = q < 64 ? (q >> 1) * 20 + (q & 1) * 4 : (q - 64) * 40 + 16, dk = q < 64 ? 8 : 20;
    const unsigned wof = b32 < 25 ? k0 * 1280 + ((h & 7) * 80 + c4 * 4) * 2 : OOB;
#pragma unroll
    for (int j = 0; j < 4; ++j) {
      r[j] = __builtin_amdgcn_raw_buffer_load_b64(rw, wof, j * 1280, 0);
      r[4 + j] = __builtin_amdgcn_raw_buffer_load_b64(rw, b32 < 25 ? wof + dk * 1280 : OOB, j * 1280, 0);
    }
    pos = lane * 64;
    dst = OFF_WO + (m2 * 8 + (h & 7)) * WO_CG + b32 * 4096;
    np = b32 < 25 ? 4 : 0;
  }
  if (bar) bar_lds();
  transpose8x4(r, o);
#pragma unroll
  for (int i = 0; i < 4; ++i) if (pos < np * 1024) lds_w128(bounce + pos + i * 16, o[i]);
#pragma unroll
  for (int i = 0; i < 4; ++i) o[i] = lds_r128(bounce + i * 1024 + lane * 16);
#pragma unroll
  for (int i = 0; i < 4; ++i) st128(o[i], rs, (unsigned)i < np ? lane * 16 : OOB, dst + i * 1024);
}

__global__ void __launch_bounds__(768, 3) main_kernel(const u16* __restrict__ x, const u16* __restrict__ wqA, const u16* __restrict__ woA,
                                                      const u16* __restrict__ wqB, const u16* __restrict__ woB,
                                                      char* __restrict__ ws, u16* __restrict__ out) {
  const int tid0 = threadIdx.x;
  const int w = __builtin_amdgcn_readfirstlane(tid0 >> 6);
  const int y = (w >> 2) & 1;
  const int tt = y ? 7 - w : w + 4;
  const int xg = blockIdx.x & 7, xi = blockIdx.x >> 3;
  const int c0 = ((xg << MAPC) & 7) | (xi & ((1 << MAPC) - 1));
  const int c = MIX ? ((c0 & 1) << 2) | (c0 >> 1) : c0;
  const int h = ((xg >> (3 - MAPC)) << (5 - MAPC)) | (xi >> MAPC);
#define LANE_VARS const int tid = opaque(tid0), lane = tid & 63, t = lane & 31, hh = lane >> 5; (void)t; (void)hh
  const rsrc_t rs = __builtin_amdgcn_make_buffer_rsrc(ws, 0, WS_USED, 0x00020000);
  const rsrc_t ro = __builtin_amdgcn_make_buffer_rsrc(out, 0, XIMG, 0x00020000);
  const u32x4 srd = {(unsigned)(size_t)ws, (unsigned)((size_t)ws >> 32) & 0xffffu, WS_USED, 0x00020000u};
  const f32x16 zero16 = {0.f, 0.f, 0.f, 0.f, 0.f, 0.f, 0.f, 0.f, 0.f, 0.f, 0.f, 0.f, 0.f, 0.f, 0.f, 0.f};
  STAMP(0);
  if (tid0 < 384) *(LAS unsigned*)(L_M + tid0 * 4) = 0u;

  if (w >= 8) {
    const int u = w - 8, lane = tid0 & 63, me = h * 8 + c;
    const unsigned l16 = lane * 16, l4 = u * 1024 + l16;
    const unsigned l0 = (u & 1) * 1024 + l16, l5 = l0 + 10240 < UNIT ? l0 : OOB;
    const u32x4 sv = {SENT, SENT, SENT, SENT};
    __builtin_amdgcn_s_setprio(HPRIO);
    u32x2 tag = ld64(rs, 0, OFF_FLAG);
    if (u == 0) {
      const rsrc_t rx = __builtin_amdgcn_make_buffer_rsrc((void*)x, 0, XIMG, 0x00020000);
      const int row = c * 256 + h * 8 + (lane & 7), p = lane >> 3;
      u32x4 v[10];
#pragma unroll
      for (int i = 0; i < 10; ++i) v[i] = __builtin_amdgcn_raw_buffer_load_b128(rx, row * 1280 + p * 16, i * 128, 0);
      st128(sv, rs, lane < 16 ? OFF_F1 + p * 32768 + me * 128 + (lane & 7) * 16 : OOB, 0);
      st128(sv, rs, OFF_KF + (((p >> 1) * 32 + h) * 16 + 2 * c + (p & 1)) * 128 + (lane & 7) * 16, 0);
      bar_lds();
      const unsigned xd = OFF_X + (row >> 5) * XT;
      float ss = 0.f, sm = 0.f;
#pragma unroll
      for (int i = 0; i < 10; ++i) {
        st128(clean4(v[i]), rs, xd + p * 512 + (row & 31) * 16, i * 4096);
        ss = sq2(v[i].x, ss); ss = sq2(v[i].y, ss); ss = sq2(v[i].z, ss); ss = sq2(v[i].w, ss);
        sm = sum2(v[i].x, sm); sm = sum2(v[i].y, sm); sm = sum2(v[i].z, sm); sm = sum2(v[i].w, sm);
      }
      st128(clean4((u32x4){__float_as_uint(sm), __float_as_uint(ss), 0u, 0u}), rs, xd + 40960 + (row & 31) * 16 + p * 512, 0);
      tag = next_tag(tag);
      drain_vm();
      ev_wait(L_C + C_W0 * 4, 3);
      st64(tag, rs, lane == 0 ? 0u : OOB, OFF_F1 + me * 128);
      ev_post(L_C + C_F1 * 4);
      STAMP(41);
      wait_tag(rs, lane < 40 ? OFF_F1 + (lane < 32 ? lane * 8 + c : h * 8 + lane - 32) * 128 : OOB, tag);
      ev_post(L_C + C_X0 * 4);
      STAMP(42);
    } else {
      if (RPOS == 0) for (unsigned q = 0; q < 10u + (u == 3); ++q) st128(sv, rs, l16, u < 3 ? o_piece(q, u - 1, c, h) : x_piece(q, 1, c, h));
      bar_lds();
      tag = next_tag(tag);
      ev_wait(L_C + C_X0 * 4, 1);
    }
    {
      u32x4 wn[20];
#pragma unroll
      for (int i = 0; i < 20; ++i) wn[i] = ld128(rs, l4, OFF_WQ + h * WQ_HEAD + i * 4096);
#pragma unroll
      for (int q = 0; q < 4; ++q) {
#pragma unroll
        for (int i = 5 * q; i < 5 * q + 5; ++i) lds_w128(L_A + l4 + i * 4096, wn[i]);
        ev_post(L_C + (C_WQ + q) * 4);
      }
      STAMP(45);
      if (RPOS == 1 && u) for (unsigned q = 0; q < 10u + (u == 3); ++q) { st128(sv, rs, l16, u < 3 ? o_piece(q, u - 1, c, h) : x_piece(q, 1, c, h)); __builtin_amdgcn_s_sleep(RSLP); }
      add_colsum(wn, u, lane, L_M);
      ev_post(L_C + C_CS * 4);
      drain_vm();
      ev_post(L_C + C_INIT * 4);
      if (u == 3) {
        ev_wait<2>(L_C + C_INIT * 4, 12);
        st64(tag, rs, lane == 0 ? 0u : OOB, OFF_F2 + me * 128);
        STAMP(43);
      }
    }
    for (int layer = 0; layer < 4; ++layer) {
      const int m = layer & 1;
      const unsigned ev = L_C + layer * 256;
      const unsigned ub = (layer * 32 + h) * 16, wo_src = OFF_WO + (m * 8 + (h & 7)) * WO_CG;
      const unsigned hi = u < 2;
      const unsigned own = m ? 1 - hi : 3 + hi;
      const unsigned wq_next = layer < 3 ? OFF_WQ + ((m ^ 1) * 32 + h) * WQ_HEAD : OOB;
      if (LNAP) ev_wait<LNAP>(ev + C_PR * 4, 1);
      if (!hi && layer == 2) {
        ev_wait<2>(ev + C_PR * 4, 8);
        for (int p = u - 2; p < WOE; p += 2) dma1k(L_A + p * 1024, l16, srd, wo_src + p * 1024);
      }
      ev_wait(ev + (C_RDY + own) * 4, 4);
      STAMP(1 + layer * 10);
      kv_publish(l0, l5, rs, ub + 2 * c + hi, own);
      drain_vm();
      st64(tag, rs, lane == 0 ? (u & 1) * 8 : OOB, OFF_KF + (ub + 2 * c + hi) * 128);
      ev_post(ev + C_PUB * 4);
      STAMP(2 + layer * 10);
      {
        const unsigned un = ub + 2 * (m ? kv_order(c, 1) : (c - 1) & 7) + hi, slot = m ? 3 - hi : 1 + hi;
        if (m || c > 0) {
          wait_tag(rs, lane < 2 ? OFF_KF + un * 128 + lane * 8 : OOB, tag);
          kv_fetch(l16, srd, un, slot, u & 1, 2);
          drain_vm();
        } else {
          kv_fake(l0, l5, slot);
        }
        ev_post(ev + (C_RDY + slot) * 4, 2);
      }
      STAMP(3 + layer * 10);
      if (m == 0) {
        if (layer == 0) {
          wait_tag(rs, OFF_F2 + (lane < 32 ? lane * 8 + c : (((lane >> 3) & 3) * 8 + (h & 7)) * 8 + (lane & 7)) * 128, tag);
          ev_post(L_C + C_SEEN * 4);
          STAMP(44);
          ev_wait(ev + C_PUB * 4, 4);
          for (int p = u; p < 100; p += 4) dma1k(L_A + p * 1024, l16, srd, wo_src + p * 1024);
        } else if (hi) {
          ev_wait(ev + (C_RDY + 3) * 4, 4);
          for (int p = WOE + u; p < 100; p += 2) dma1k(L_A + p * 1024, l16, srd, wo_src + p * 1024);
        }
      } else {
        ev_wait(ev + C_PUB * 4, 4);
        for (int n = 4 + u; n < 16; n += 4) {
          const unsigned un2 = ub + 2 * kv_order(c, n >> 1) + 1 - (n & 1);
          wait_tag(rs, lane < 2 ? OFF_KF + un2 * 128 + lane * 8 : OOB, tag);
          if (n >= 5) ev_wait<HNAP>(ev + (C_DONE + n - 5) * 4, 8);
          kv_fetch(l16, srd, un2, n % 5, 0, 1);
          drain_vm();
          ev_post(ev + (C_RDY + n) * 4, 4);
          if (n < 8) {
            for (int p = u; p < 100; p += 4) { dma1k(L_A + p * 1024, l16, srd, wo_src + p * 1024); __builtin_amdgcn_s_sleep(1); }
          } else if (n < 12 && layer == 1) {
            for (unsigned q = 0; q < 10u + (u >> 1); ++q) {
              st128(sv, rs, l16, u < 2 ? o_piece(q, 2 + u, c, h) : x_piece(q, u, c, h));
              __builtin_amdgcn_s_sleep(2);
            }
          }
        }
        if (layer == 3) {
          ev_wait<4>(ev + (C_DONE + 15) * 4, 1);
          st64(tag, rs, (tid0 == 512 && blockIdx.x == 0) ? 0u : OOB, OFF_FLAG);
        }
      }
      drain_vm();
      ev_post(ev + C_WO * 4);
      STAMP(6 + layer * 10);
      if (layer == 3) break;
      if (m == 0 && LNAP) ev_wait<4>(ev + C_OS * 4, 8);
      if (m == 0 && (hi || !LOE || layer == 0)) ev_wait<2>(ev + C_OL * 4, OLN);
      u32x4 wn[20];
#pragma unroll
      for (int i = 0; i < 20; ++i) wn[i] = ld128(rs, l4, wq_next + i * 4096);
      SB();
      STAMP(7 + layer * 10);
      if (m) { ev_wait<16>(ev + (C_DONE + 15) * 4, 4); if (LNAP) ev_wait<4>(ev + C_OL * 4, 4); }
      ev_wait(ev + C_OP * 4, 8);
      STAMP(8 + layer * 10);
#pragma unroll
      for (int q = 0; q < 4; ++q) {
#pragma unroll
        for (int i = 5 * q; i < 5 * q + 5; ++i) lds_w128(L_A + l4 + i * 4096, wn[i]);
        if (q == 3) { ev_wait(ev + C_XG * 4, STREAM ? 4 : 8); ev_wait(ev + (C_XG + STREAM) * 4, 4); }
        ev_post(ev + 256 + (C_WQ + q) * 4);
      }
      if (layer == 0) { add_colsum(wn, u, lane, L_M + 256); ev_post(ev + 256 + C_CS * 4); }
      STAMP(9 + layer * 10);
    }
    STAMP_OUT();
    return;
  }

  {
    const int lane = tid0 & 63;
    if (w < 3) {
      weight_share(w, lane, c, h, wqA, woA, rs, L_U + w * 4096, true);
      drain_vm();
      ev_post(L_C + C_W0 * 4);
    } else {
      const unsigned id = (blockIdx.x * 5 + w - 3) * 64 + lane;
      const rsrc_t r1 = __builtin_amdgcn_make_buffer_rsrc((void*)wqB, 0, 640 * 3840, 0x00020000);
      const rsrc_t r2 = __builtin_amdgcn_make_buffer_rsrc((void*)woA, 0, 640 * 1280, 0x00020000);
      const rsrc_t r3 = __builtin_amdgcn_make_buffer_rsrc((void*)woB, 0, 640 * 1280, 0x00020000);
      const u32x4 p0 = __builtin_amdgcn_raw_buffer_load_b128(r1, id * 16, 0, 0), p1 = __builtin_amdgcn_raw_buffer_load_b128(r1, id * 16, 1310720, 0);
      const u32x4 p2 = __builtin_amdgcn_raw_buffer_load_b128(r2, id * 16, 0, 0), p3 = __builtin_amdgcn_raw_buffer_load_b128(r3, id * 16, 0, 0);
      bar_lds();
      asm volatile("" :: "v"(p0), "v"(p1), "v"(p2), "v"(p3));
      ev_wait(L_C + C_F1 * 4, 1);
      weight_share(w, lane, c, h, wqB, w == 3 ? woA : woB, rs, L_U + w * 4096, false);
    }
    STAMP(41);
    ev_wait(L_C + C_X0 * 4, 1);
    if (X0SLP) __builtin_amdgcn_s_sleep(X0SLP);
    STAMP(42);
  }

  for (int layer = 0; layer < 4; ++layer) {
    const int m = layer & 1;
    const unsigned ev = L_C + layer * 256;
    u32x4 q1, k1; u32x2 q2, k2;
    {
      LANE_VARS;
      __builtin_amdgcn_s_setprio(GPRI);
      STAMP(1 + layer * 10);
      const unsigned xoff = OFF_X + layer * XIM2 + (c * 8 + tt) * XT;
      const unsigned xl = lane * 16;
#define XLD(k) ld128(rs, xl + ((k) & 3) * 1024, xoff + ((k) >> 2) * 4096)
#define XISS(ks) (XI0 + 2 * (ks) < XD + (ks) ? XI0 + 2 * (ks) : XD + (ks))
      u32x4 xb[40];
      u32x4 sq[4];
#pragma unroll
      for (int ks = 0; ks < XI0; ++ks) xb[ks] = XLD(ks);
      SB();
      const unsigned wa = opaque(L_A + lane * 16), wb = opaque(L_A + 40960 + lane * 16);
      u32x4 wf[3][2];
      ev_wait(ev + C_WQ * 4, 4);
#pragma unroll
      for (int ks = 0; ks < 2; ++ks) { wf[ks][0] = lds_r128(wa + ks * 2048); wf[ks][1] = lds_r128(wa + ks * 2048 + 1024); }
      f32x16 a0 = zero16, a1 = zero16;
      unsigned seen = 0;
      float rstd = 0.f;
      u32x4 cb, ca0, ca1;
#define SQLD(i) ld128(rs, t * 16 + hh * 2048 + (i) * 512, xoff + 40960)
#define SQMAX umax4(umax4(umax4(umax4(0u, sq[0]), sq[1]), sq[2]), sq[3])
#pragma unroll
      for (int g = 0; g < 8; ++g) {
        unsigned mx = 0;
#pragma unroll
        for (int i = 0; i < 5; ++i) mx = umax4(mx, xb[5 * g + i]);
        if (g == 7) mx = umax(mx, SQMAX);
        if (__builtin_expect(unwritten(mx), 0)) {
          unsigned spins = 0;
          do {
            if (give_up(spins)) break;
#pragma unroll
            for (int i = 0; i < 5; ++i) xb[5 * g + i] = XLD(5 * g + i);
            mx = 0;
            if (g == 7) {
#pragma unroll
              for (int i = 0; i < 4; ++i) sq[i] = SQLD(i);
              mx = SQMAX;
            }
#pragma unroll
            for (int i = 0; i < 5; ++i) mx = umax4(mx, xb[5 * g + i]);
          } while (unwritten(mx));
#pragma unroll
          for (int i = 5 * g + 5; i < 40; ++i) if (i < XISS(5 * g)) xb[i] = XLD(i);
        }
        if (g == 0) { ev_post(ev + C_INIT * 4); STAMP(10 + layer * 10); }
#pragma unroll
        for (int i = 0; i < 5; ++i) {
          const int ks = 5 * g + i, kn = ks + 2, kx = XISS(ks), ky = XISS(ks + 1) - 1;
          if (kn < 40 && kn % 10 == 8) seen = ev_peek(kn < 38 ? ev + (C_WQ + (kn + 2) / 10) * 4 : L_C);
          if (kn < 40 && kn % 10 == 0) ev_wait_from(seen, ev + (C_WQ + kn / 10) * 4, 4);
          if (kn == 32) seen = ev_peek(L_C + m * 256 + C_CS * 4);
          if (kn == 34) {
            ev_wait_from(seen, L_C + m * 256 + C_CS * 4, 4);
            unsigned h0, m0, l0, h1, m1, l1;
            split3(*(LAS float*)(L_M + m * 256 + t * 4), h0, m0, l0);
            split3(*(LAS float*)(L_M + m * 256 + 128 + t * 4), h1, m1, l1);
            ca0 = (u32x4){(h0 >> 16) | h0, (m0 >> 16) | m0, (h0 >> 16) | l0, 0u};
            ca1 = (u32x4){(h1 >> 16) | h1, (m1 >> 16) | m1, (h1 >> 16) | l1, 0u};
          }
          if (kn < 40) {
            const unsigned ad = kn < 20 ? wa + kn * 2048 : wb + (kn - 20) * 2048;
            wf[kn % 3][0] = lds_r128(ad); wf[kn % 3][1] = lds_r128(ad + 1024);
          }
          SB();
          a0 = mfma32(wf[ks % 3][0], xb[ks], a0);
          if (ky > kx && kx < 40) xb[kx] = XLD(kx);
          if (ks == 36) {
            const f32x4 st = (__builtin_bit_cast(f32x4, sq[0]) + __builtin_bit_cast(f32x4, sq[1])) + (__builtin_bit_cast(f32x4, sq[2]) + __builtin_bit_cast(f32x4, sq[3]));
            float sm = st[0] + st[2], ss = st[1] + st[3];
            sm = half_lo(sm) + half_hi(sm);
            ss = half_lo(ss) + half_hi(ss);
            const float mean = sm * (1.f / 640.f);
            rstd = __builtin_amdgcn_rsqf(fmaxf(ss * (1.f / 640.f) - mean * mean, 0.f) + 1e-5f);
            unsigned mh, mm, ml;
            split3(hh ? 0.f : -mean, mh, mm, ml);
            cb = (u32x4){(mh >> 16) | mm, (mh >> 16) | mm, (ml >> 16) | mh, 0u};
          }
          SB();
          a1 = mfma32(wf[ks % 3][1], xb[ks], a1);
          if (ky < 40) xb[ky] = XLD(ky);
          if (ky >= 40 && ky < 44) sq[ky - 40] = SQLD(ky - 40);
          if (ks == 28) ev_post(ev + C_PR * 4);
          SB();
        }
      }
      a1 = mfma32(ca1, cb, a1);
      a0 = mfma32(ca0, cb, a0);
      const unsigned own = m ? y : 4 - y;
      const unsigned tb = L_U + own * UNIT + (tt & 3) * TILE;
      const unsigned vb = tb + T_V + (t >> 3) * VOCT + (t & 7) * 2;
      const unsigned vA = vb + hh * 64, vB = vb + (hh ? 14 : 8) * 16, vC = vb + (hh ? 18 : 12) * 16;
      const float rq = rstd * (0.22360679774997896f * 1.4426950408889634f), r2 = hh ? rstd : rq;
      SB();
      STAMP(2 + layer * 10);
      const unsigned c0 = pack2(a1[8] * rstd, a1[9] * rstd), c1 = pack2(a1[10] * rstd, a1[11] * rstd);
      const unsigned d0 = pack2(a1[12] * rstd, a1[13] * rstd), d1 = pack2(a1[14] * rstd, a1[15] * rstd);
      k1 = (u32x4){pack2(a1[0] * rstd, a1[1] * rstd), pack2(a1[2] * rstd, a1[3] * rstd), pack2(a1[4] * rstd, a1[5] * rstd), pack2(a1[6] * rstd, a1[7] * rstd)};
      k2 = (u32x2){hh ? 0x3F803F80u : c0, hh ? 0x00003F80u : c1};
      lds_w128(tb + lane * 16, k1);
      lds_w64(tb + T_K2 + lane * 8, k2);
      const unsigned cc = hh ? c0 : 0x3F800000u;
      lds_w16lo(vC, cc); lds_w16hi(vC + 16, cc); lds_w16lo(vC + 32, c1); lds_w16hi(vC + 48, c1);
      const unsigned e0 = pack2(a0[8] * r2, a0[9] * r2), e1 = pack2(a0[10] * r2, a0[11] * r2);
      const unsigned f0 = pack2(a0[12] * rstd, a0[13] * rstd), f1 = pack2(a0[14] * rstd, a0[15] * rstd);
      const unsigned b0 = hh ? e0 : d0, b1 = hh ? e1 : d1;
      asm volatile("" ::: "memory");
      lds_w16lo(vB, b0); lds_w16hi(vB + 16, b0); lds_w16lo(vB + 32, b1); lds_w16hi(vB + 48, b1);
      lds_w16lo(vA, f0); lds_w16hi(vA + 16, f0); lds_w16lo(vA + 32, f1); lds_w16hi(vA + 48, f1);
      ev_post(ev + (C_RDY + own) * 4);
      q1 = (u32x4){pack2(a0[0] * rq, a0[1] * rq), pack2(a0[2] * rq, a0[3] * rq), pack2(a0[4] * rq, a0[5] * rq), pack2(a0[6] * rq, a0[7] * rq)};
      q2 = (u32x2){hh ? 0u : e0, hh ? 0u : e1};
      __builtin_amdgcn_s_setprio(0);
    }
    STAMP(3 + layer * 10);

    {
      LANE_VARS;
      const unsigned kvl = OFF_KV + layer * KV_LAYER + h * 16 * UNIT;
      const unsigned vl = T_V + hh * VOCT + vpos(t) * 16;
      const unsigned lk = L_U + lane * 16, lk2 = L_U + T_K2 + lane * 8, lv = L_U + vl;
      f32x16 o = zero16, sa, sb = zero16;
      Frag f;
      f.vb = (u32x4){0u, 0u, 0u, 0u};
      float mx;
      if (m == 1) {
        sa = mfma32k8(k2, q2, mfma32(k1, q1, zero16));
        SB();
        ev_wait(ev + C_RDY * 4, 4);
        const u32x4 g1 = lds_r128(lk);
        const u32x2 g2 = lds_r64(lk2);
        f.va = lds_r128(lv);
        f.k1 = lds_r128(lk + TILE);
        f.k2 = lds_r64(lk2 + TILE);
        SB();
        mx = max16(sa);
        mx = fmaxf(half_lo(mx), half_hi(mx));
        set_shift(q2, mx, hh);
        sa = mfma32k8(g2, q2, mfma32(g1, q1, zero16));
        STAMP(4 + layer * 10);
        unsigned kb = lk, k2b = lk2, vc = lv, nxt = UNIT;
        if (YPRI == 4) prio_if(y);
        for (int n = 0; n < 16; ++n) {
          if (YPRI >= 1 && YPRI < 4) prio_if(y);
          att_step<1, 1, 1>(sa, sb, o, f, q1, q2, zero16, vc + 2 * VOCT, kb + 2 * TILE, k2b + 2 * TILE, vc + TILE);
          if (YPRI == 1) __builtin_amdgcn_s_setprio(0);
          const unsigned seen = ev_peek(ev + (C_RDY + n + 1) * 4);
          att_step<1, 1, 1>(sb, sa, o, f, q1, q2, zero16, vc + TILE + 2 * VOCT, kb + 3 * TILE, k2b + 3 * TILE, vc + 2 * TILE);
          ev_wait_from<ONAP>(seen, ev + (C_RDY + n + 1) * 4, n < 15 ? 4 : 0);
          kb = lk + nxt; k2b = lk2 + nxt;
          const unsigned vn = lv + nxt;
          if (YPRI == 2) __builtin_amdgcn_s_setprio(0);
          att_step<1, 1, 1>(sa, sb, o, f, q1, q2, zero16, vc + 2 * TILE + 2 * VOCT, kb, k2b, vc + 3 * TILE);
          if (YPRI == 3) __builtin_amdgcn_s_setprio(0);
          att_step<1, 1, 1>(sb, sa, o, f, q1, q2, zero16, vc + 3 * TILE + 2 * VOCT, kb + TILE, k2b + TILE, vn);
          ev_post(ev + (C_DONE + n) * 4);
          vc = vn;
          nxt = nxt == 4 * UNIT ? 0 : nxt + UNIT;
        }
        o = pv_hi(f.vb, sb, o);
      } else {
        sa = mfma32k8(k2, q2, mfma32(k1, q1, tri_mask<true>()));
        const f32x16 far = tri_mask<false>();
        const unsigned ka = lk + (4 + tt) * TILE, k2a = lk2 + (4 + tt) * TILE, va = lv + (4 + tt) * TILE;
        f.va = lds_r128(va + 8 * TILE);
        const unsigned sn = ev_peek(ev + (C_RDY + 4 - y) * 4);
        SB();
        mx = max16(sa);
        mx = fmaxf(half_lo(mx), half_hi(mx));
        set_shift(q2, mx, hh);
#pragma unroll
        for (int i = 0; i < 16; ++i) sa[i] -= mx;
        SB();
        ev_wait_from(sn, ev + (C_RDY + 4 - y) * 4, 4);
        const int x1 = tt & 3;
        const unsigned e1 = ev + (C_RDY + 3 - y) * 4, e2 = e1 - 4;
        ev_wait_if_eq(x1, 0, e1, 4);
        STAMP(4 + layer * 10);
        f.k1 = lds_r128(ka + 7 * TILE);
        f.k2 = lds_r64(k2a + 7 * TILE);
#define STEP_W(n, A, B) ev_wait_if_eq(x1, ((n) + 1) & 3, (n) < 3 ? e1 : e2, 4); \
        att_step<1, 1, 1>(A, B, o, f, q1, q2, zero16, va + (8 - (n)) * TILE + 2 * VOCT, ka + (6 - (n)) * TILE, k2a + (6 - (n)) * TILE, va + (7 - (n)) * TILE)
        STEP_W(0, sa, sb);
        STEP_W(1, sb, sa);
        STEP_W(2, sa, sb);
        STEP_W(3, sb, sa);
        STEP_W(4, sa, sb);
        STEP_W(5, sb, sa);
        STEP_W(6, sa, sb);
        att_step<1, 0, 1>(sb, sa, o, f, q1, q2, far, va + TILE + 2 * VOCT, 0, 0, va);
        att_step<0, 0, 0>(sa, sb, o, f, q1, q2, zero16, va + 2 * VOCT, 0, 0, 0);
        o = pv_hi(f.vb, sa, o);
      }
      __builtin_amdgcn_s_setprio(2);
      STAMP(5 + layer * 10);
      const float tot = ((o[0] + o[1]) + (o[2] + o[3])) + ((o[4] + o[5]) + (o[6] + o[7])) + ((o[8] + o[9]) + (o[10] + o[11]));
      const bool bad = !(fabsf(tot) <= 3.0e38f) | !(o[8] <= (hh ? SUM_LIMIT : 3.0e38f));
      if (__builtin_expect(__builtin_amdgcn_ballot_w64(bad) != 0, 0)) {
        const int qt = c * 8 + tt;
        const int lo = m ? 0 : (qt >= 8 ? qt - 8 : 0), hi = m ? 63 : qt;
        ev_wait(ev + C_PUB * 4, 4);
        if (hh) { q2.x = 0u; q2.y = 0u; }
        mx = -3.0e38f;
        for (int pass = 0; pass < 2; ++pass) {
          o = zero16;
          for (int kt = lo; kt <= hi; ++kt) {
            const unsigned so = kvl + kt * TILE;
            const u32x4 j1 = ld128(rs, lane * 16, so), v0 = ld128(rs, vl, so), v1 = ld128(rs, vl + 2 * VOCT, so);
            const u32x2 j2 = ld64(rs, lane * 8 + T_K2, so);
            f32x16 cin = zero16;
            if (!m && kt == qt) cin = tri_mask<true>();
            if (!m && kt == qt - 8) cin = tri_mask<false>();
            f32x16 s = mfma32k8(j2, q2, mfma32(j1, q1, cin));
            if (pass == 0) {
              mx = fmaxf(mx, max16(s));
            } else {
#pragma unroll
              for (int e = 0; e < 16; ++e) s[e] = __builtin_amdgcn_exp2f(s[e]);
              o = pv_lo(v0, s, o);
              o = pv_hi(v1, s, o);
            }
          }
          if (pass == 0) { mx = fmaxf(half_lo(mx), half_hi(mx)); set_shift(q2, mx, hh); }
        }
      }
      const float inv = __builtin_amdgcn_rcpf(half_hi(o[8]));
      const u32x4 p = {pack2(o[0] * inv, o[1] * inv), pack2(o[2] * inv, o[3] * inv), pack2(o[4] * inv, o[5] * inv), pack2(o[6] * inv, o[7] * inv)};
      const unsigned oo = OFF_O + layer * XIMG + (c * 16 + tt * 2) * 20480;
      const unsigned lo = (t >> 4) * 20480 + (t & 15) * 16;
      ev_wait_if_eq(layer, 0, L_C + C_SEEN * 4, 1);
      st128(clean4(p), rs, lo + hh * 256, oo + h * 512);
      st64(clean2((u32x2){pack2(o[8] * inv, o[9] * inv), pack2(o[10] * inv, o[11] * inv)}), rs, hh ? OOB : lo, oo + (64 + (h >> 1)) * 256 + (h & 1) * 8);
    }
    STAMP(6 + layer * 10);

    {
      LANE_VARS;
      const int cg = h & 7, t16 = lane & 15, g4 = lane >> 4, kh = (w >> 1) & 1;
      const int rt = c * 16 + ((h >> 3) + 4 - 4 * y) * 2 + (w & 1);
      const int row = rt * 16 + t16;
      const unsigned xl = (row >> 5) * XT + (row & 31) * 16 + cg * 5120;
      const unsigned xo = OFF_X + layer * XIM2;
      const unsigned xa = xl + (4 * kh + g4) * 512, xc = kh ? OOB : xl + (8 + (g4 >> 1)) * 512 + (g4 & 1) * 8;
      const u32x4 ra = ld128(rs, xa, xo);
      const u32x2 rc = ld64(rs, xc, xo);
      const unsigned oo = OFF_O + layer * XIMG + rt * 20480 + kh * 10240;
      const unsigned ol = lane * 16;
#define OLD(k) ld128(rs, ol + ((k) & 3) * 1024, oo + ((k) >> 2) * 4096)
      u32x4 ob[10];
      const unsigned fe = ev + (m ? C_DONE + 11 + y : C_OS + STREAM * y) * 4, ft = m ? 8 : 4;
      unsigned fr = 0;
      const unsigned wa0 = opaque(L_A + kh * 51200 + lane * 16);
      constexpr int WD = OWD;
      u32x4 wf[WD + 1];
      if (ODRAIN) drain_vm();
      ev_post(ev + (C_OS + STREAM * y) * 4);
      f32x4 acc[5];
#pragma unroll
      for (int n = 0; n < 5; ++n) acc[n] = (f32x4){0.f, 0.f, 0.f, 0.f};
      ev_wait(ev + (C_OS + STREAM * y) * 4, STREAM ? 4 : 8);
      if (OSLP) __builtin_amdgcn_s_sleep(OSLP);
#pragma unroll
      for (int ks = 0; ks < 10; ++ks) ob[ks] = OLD(ks);
      SB();
      if (OLI) ev_post(ev + C_OL * 4);
      ev_wait(ev + C_WO * 4, 4);
#pragma unroll
      for (int i = 0; i < WD; ++i) wf[i] = lds_r128(wa0 + i * 1024);
#pragma unroll
      for (int g = 0; g < 2; ++g) {
        unsigned mx = 0;
#pragma unroll
        for (int i = 0; i < 5; ++i) mx = umax4(mx, ob[5 * g + i]);
        if (__builtin_expect(unwritten(mx), 0)) {
          unsigned spins = 0;
          do {
            if (give_up(spins)) break;
#pragma unroll
            for (int i = 5 * g; i < 10; ++i) ob[i] = OLD(i);
            mx = 0;
#pragma unroll
            for (int i = 0; i < 5; ++i) mx = umax4(mx, ob[5 * g + i]);
          } while (unwritten(mx));
        }
        if (g == 0) { if (!OLI) ev_post(ev + C_OL * 4); STAMP(7 + layer * 10); }
        if (g == 1) fr = ev_peek(fe);
#pragma unroll
        for (int e = 0; e < 25; ++e) {
          const int i = 25 * g + e, in = i + WD;
          if (in < 50) wf[in % (WD + 1)] = lds_r128(wa0 + in * 1024);
          acc[i % 5] = mfma16(wf[i % (WD + 1)], ob[i / 5], acc[i % 5]);
          SB();
        }
      }
      STAMP(8 + layer * 10);
      const unsigned sc = L_U + (m + y) * UNIT + (w & 1) * 5120 + lane * 16;
      ev_post(ev + C_OP * 4);
      ev_wait_from(fr, fe, ft);
      const unsigned pe = ev + (C_RED + y * 2 + (w & 1)) * 4;
      f32x4 u0, u1;
      if (kh) {
        *(LAS f32x4*)(sc) = acc[0]; *(LAS f32x4*)(sc + 1024) = acc[1]; *(LAS f32x4*)(sc + 4096) = acc[4];
        u0 = acc[2]; u1 = acc[3];
      } else {
        *(LAS f32x4*)(sc + 2048) = acc[2]; *(LAS f32x4*)(sc + 3072) = acc[3];
        u0 = acc[0]; u1 = acc[1];
      }
      ev_post(pe);
      u0 += (f32x4){bf_lo(ra.x), bf_hi(ra.x), bf_lo(ra.y), bf_hi(ra.y)};
      u1 += (f32x4){bf_lo(ra.z), bf_hi(ra.z), bf_lo(ra.w), bf_hi(ra.w)};
      f32x4 u2 = acc[4] + (f32x4){bf_lo(rc.x), bf_hi(rc.x), bf_lo(rc.y), bf_hi(rc.y)};
      ev_wait<0>(pe, 2);
      const f32x4 p0 = *(LAS f32x4*)(sc + kh * 2048), p1 = *(LAS f32x4*)(sc + kh * 2048 + 1024), p2 = *(LAS f32x4*)(sc + 4096);
      SB();
      u0 += p0; u1 += p1; u2 += p2;
      const u32x4 na = {pack2(u0[0], u0[1]), pack2(u0[2], u0[3]), pack2(u1[0], u1[1]), pack2(u1[2], u1[3])};
      u32x2 nc = {pack2(u2[0], u2[1]), pack2(u2[2], u2[3])};
      if (layer < 3) {
        SB();
        st128(clean4(na), rs, xa, xo + XIM2);
        st64(clean2(nc), rs, xc, xo + XIM2);
        ev_post(ev + (C_XG + STREAM * y) * 4);
        SB();
        STAMP(50 + layer);
        if (kh) nc = (u32x2){0u, 0u};
        float s0 = sq2(na.x, 0.f), s1 = sum2(na.x, 0.f);
        s0 = sq2(na.y, s0); s1 = sum2(na.y, s1); s0 = sq2(na.z, s0); s1 = sum2(na.z, s1); s0 = sq2(na.w, s0); s1 = sum2(na.w, s1);
        s0 = sq2(nc.x, s0); s1 = sum2(nc.x, s1); s0 = sq2(nc.y, s0); s1 = sum2(nc.y, s1);
        s0 = half_lo(s0) + half_hi(s0);
        s1 = half_lo(s1) + half_hi(s1);
        const auto q0 = __builtin_amdgcn_permlane16_swap(__float_as_uint(s0), __float_as_uint(s0), false, false);
        const auto q1 = __builtin_amdgcn_permlane16_swap(__float_as_uint(s1), __float_as_uint(s1), false, false);
        s0 = __uint_as_float(q0[0]) + __uint_as_float(q0[1]);
        s1 = __uint_as_float(q1[0]) + __uint_as_float(q1[1]);
        st64(clean2((u32x2){__float_as_uint(s1), __float_as_uint(s0)}), rs,
             lane < 16 ? (row >> 5) * XT + 40960 + (row & 31) * 16 + cg * 512 + kh * 8 : OOB, xo + XIM2);
      } else {
        const unsigned od = row * 1280 + cg * 160;
        st128a<0>(na, ro, od + kh * 64 + g4 * 16, 0);
        __builtin_amdgcn_raw_buffer_store_b64(nc, ro, kh ? OOB : od + 128 + g4 * 8, 0, 0);
      }
      if (layer < 3) { ev_wait(ev + (C_XG + STREAM * y) * 4, STREAM ? 4 : 8); if (XSLP) __builtin_amdgcn_s_sleep(XSLP); }
    }
    STAMP(9 + layer * 10);
  }
  STAMP_OUT();
}

extern "C" void kernel_launch(void* const* d_in, const int* in_sizes, int n_in, void* d_out, int out_size, void* d_ws, size_t ws_size,
                              hipStream_t stream) {
  (void)in_sizes; (void)n_in; (void)out_size; (void)ws_size;
  (void)hipFuncSetAttribute((const void*)main_kernel, hipFuncAttributeMaxDynamicSharedMemorySize, LDS_BYTES);
  main_kernel<<<256, 768, LDS_BYTES, stream>>>((const u16*)d_in[0], (const u16*)d_in[1], (const u16*)d_in[2], (const u16*)d_in[4],
                                               (const u16*)d_in[5], (char*)d_ws, (u16*)d_out);
}
```

```cpp
#include <hip/hip_runtime.h>
#include <hip/hip_bf16.h>

typedef float f32x16 __attribute__((ext_vector_type(16)));
typedef float f32x8 __attribute__((ext_vector_type(8)));
typedef float f32x4 __attribute__((ext_vector_type(4)));
typedef unsigned u32x4 __attribute__((ext_vector_type(4)));
typedef unsigned u32x2 __attribute__((ext_vector_type(2)));
typedef __bf16 bf16x16 __attribute__((ext_vector_type(16)));
typedef __bf16 bf16x8 __attribute__((ext_vector_type(8)));
typedef __bf16 bf16x2 __attribute__((ext_vector_type(2)));
typedef short s16x4 __attribute__((ext_vector_type(4)));
typedef unsigned short u16;

#define LAS __attribute__((address_space(3)))
typedef unsigned long long u64;
#define DEVI __device__ __forceinline__

constexpr unsigned WQ_HEAD = 81920;
constexpr unsigned WO_CG = 102400;
constexpr unsigned XIMG = 2621440;
constexpr unsigned XT = 45056, XIM2 = 64 * XT;
constexpr unsigned VOCT = 352;
constexpr unsigned TILE = 2944, T_K2 = 1024, T_V = 1536;
constexpr unsigned UNIT = 4 * TILE;
constexpr unsigned KV_LAYER = 32 * 64 * TILE;
constexpr unsigned OFF_FLAG = 0;
constexpr unsigned OFF_F1 = 4096, OFF_F2 = OFF_F1 + 32768;
constexpr unsigned OFF_KF = OFF_F2 + 32768;
constexpr unsigned OFF_WQ = OFF_KF + 4 * 32 * 16 * 128;
constexpr unsigned OFF_WO = OFF_WQ + 2 * 32 * WQ_HEAD;
constexpr unsigned OFF_X = OFF_WO + 2 * 8 * WO_CG;
constexpr unsigned OFF_O = OFF_X + 4 * XIM2;
constexpr unsigned OFF_KV = OFF_O + 4 * XIMG;
constexpr unsigned WS_USED = OFF_KV + 4 * KV_LAYER;
constexpr unsigned SENT = 0xFFFFFFFFu;
constexpr unsigned OOB = 0xE0000000u;

constexpr unsigned L_A = 0;
constexpr unsigned L_U = 102400;
constexpr unsigned L_M = L_U + 5 * UNIT;
constexpr unsigned L_C = L_M + 512;
constexpr unsigned LDS_BYTES = 163840;
enum {
  C_RDY = 0, C_DONE = 16,
  C_WO = 32, C_OP = 33, C_WQ = 34, C_RED = 38,
  C_PR = 47, C_PUB = 53, C_OL = 55, C_OS = 56,
  C_W0 = 48, C_X0 = 49, C_INIT = 50, C_SEEN = 51, C_CS = 52, C_F1 = 54
};

constexpr unsigned kSpinCap = 1u << 15;
constexpr float SUM_LIMIT = 1.0e18f;
constexpr int XI0 = 8, XD = 12;
constexpr int OWD = 16;
constexpr int WOE = 40;

DEVI unsigned pack2(float a, float b) {
  bf16x2 v = {(__bf16)a, (__bf16)b};
  return __builtin_bit_cast(unsigned, v);
}
DEVI float sq2(unsigned u, float acc) {
  const bf16x2 p = __builtin_bit_cast(bf16x2, u);
  return __builtin_amdgcn_fdot2_f32_bf16(p, p, acc, false);
}
DEVI float bf_lo(unsigned u) { return __uint_as_float(u << 16); }
DEVI float bf_hi(unsigned u) { return __uint_as_float(u & 0xffff0000u); }
DEVI f32x16 mfma32(u32x4 a, u32x4 b, f32x16 c) {
  return __builtin_amdgcn_mfma_f32_32x32x16_bf16(__builtin_bit_cast(bf16x8, a), __builtin_bit_cast(bf16x8, b), c, 0, 0, 0);
}
DEVI f32x16 mfma32k8(u32x2 a, u32x2 b, f32x16 c) {
  return __builtin_amdgcn_mfma_f32_32x32x8bf16_1k(__builtin_bit_cast(s16x4, a), __builtin_bit_cast(s16x4, b), c, 0, 0, 0);
}
DEVI f32x4 mfma16(u32x4 a, u32x4 b, f32x4 c) {
  return __builtin_amdgcn_mfma_f32_16x16x32_bf16(__builtin_bit_cast(bf16x8, a), __builtin_bit_cast(bf16x8, b), c, 0, 0, 0);
}
DEVI f32x16 pv_lo(u32x4 v, const f32x16& p, f32x16 o) {
  f32x8 h = __builtin_shufflevector(p, p, 0, 1, 2, 3, 4, 5, 6, 7);
  return __builtin_amdgcn_smfmac_f32_32x32x32_bf16(__builtin_bit_cast(bf16x8, v), __builtin_bit_cast(bf16x16, h), o, 0xDDDD, 0, 0);
}
DEVI f32x16 pv_hi(u32x4 v, const f32x16& p, f32x16 o) {
  f32x8 h = __builtin_shufflevector(p, p, 8, 9, 10, 11, 12, 13, 14, 15);
  return __builtin_amdgcn_smfmac_f32_32x32x32_bf16(__builtin_bit_cast(bf16x8, v), __builtin_bit_cast(bf16x16, h), o, 0xDDDD, 0, 0);
}
DEVI u32x4 lds_r128(unsigned a) { return *(LAS u32x4*)a; }
DEVI u32x2 lds_r64(unsigned a) { return *(LAS u32x2*)a; }
DEVI void lds_w128(unsigned a, u32x4 v) { *(LAS u32x4*)a = v; }
DEVI void lds_w64(unsigned a, u32x2 v) { *(LAS u32x2*)a = v; }
DEVI float half_lo(float v) { return __uint_as_float(__builtin_amdgcn_permlane32_swap(__float_as_uint(v), __float_as_uint(v), false, false)[0]); }
DEVI float half_hi(float v) { return __uint_as_float(__builtin_amdgcn_permlane32_swap(__float_as_uint(v), __float_as_uint(v), false, false)[1]); }
DEVI unsigned umax(unsigned a, unsigned b) { return a > b ? a : b; }
DEVI unsigned umax4(unsigned m, u32x4 v) { return umax(umax(umax(umax(m, v.x), v.y), v.z), v.w); }
DEVI bool unwritten(unsigned m) { return __builtin_amdgcn_ballot_w64(m == SENT) != 0; }
DEVI unsigned clean(unsigned v) { return v < SENT ? v : SENT - 1; }
DEVI u32x4 clean4(u32x4 v) { return (u32x4){clean(v.x), clean(v.y), clean(v.z), clean(v.w)}; }
DEVI u32x2 clean2(u32x2 v) { return (u32x2){clean(v.x), clean(v.y)}; }
DEVI unsigned vpos(int d) {
  const unsigned q = ((14u | 0u << 5 | 4u << 10 | 18u << 15 | 8u << 20) >> ((d >> 2) * 5 & 31)) & 31u;
  return d < 20 ? q + (d & 3) : (d == 20 ? 13u : 12u);
}
DEVI void lds_w16lo(unsigned a, unsigned v) { *(LAS u16*)a = (u16)v; }
DEVI void lds_w16hi(unsigned a, unsigned v) { *(LAS u16*)a = (u16)(v >> 16); }
DEVI float sum2(unsigned u, float acc) {
  const bf16x2 ones = {(__bf16)1.0f, (__bf16)1.0f};
  return __builtin_amdgcn_fdot2_f32_bf16(__builtin_bit_cast(bf16x2, u), ones, acc, false);
}
DEVI void split3(float v, unsigned& hi, unsigned& mid, unsigned& lo) {
  hi = __float_as_uint(v) & 0xffff0000u;
  const float r1 = v - __uint_as_float(hi);
  mid = __float_as_uint(r1) & 0xffff0000u;
  lo = __float_as_uint(r1 - __uint_as_float(mid)) & 0xffff0000u;
}
#define SB() __builtin_amdgcn_sched_barrier(0)
DEVI void bar_lds() {
  asm volatile("s_waitcnt lgkmcnt(0)\n\ts_barrier" ::: "memory");
}
DEVI void drain_vm() { asm volatile("s_waitcnt vmcnt(0)" ::: "memory"); }
#define DMA_ASM(d, v, r, o) "s_mov_b32 m0, " d "\n\ts_nop 3\n\tbuffer_load_dwordx4 " v ", " r ", " o " offen sc1 lds\n\t"
DEVI void dma1k(unsigned lds_dst, unsigned voff, u32x4 srd, unsigned soff) {
  unsigned keep;
  asm volatile("s_mov_b32 %0, m0\n\t" DMA_ASM("%1", "%2", "%3", "%4") "s_mov_b32 m0, %0"
               : "=&s"(keep) : "s"(__builtin_amdgcn_readfirstlane(lds_dst)), "v"(voff), "s"(srd), "s"(__builtin_amdgcn_readfirstlane(soff)) : "memory");
}
typedef __amdgpu_buffer_rsrc_t rsrc_t;
DEVI u32x4 ld128(rsrc_t r, unsigned voff, unsigned soff) { return __builtin_amdgcn_raw_buffer_load_b128(r, voff, soff, 16); }
DEVI u32x2 ld64(rsrc_t r, unsigned voff, unsigned soff) { return __builtin_amdgcn_raw_buffer_load_b64(r, voff, soff, 16); }
template <int AUX> DEVI void st128a(u32x4 v, rsrc_t r, unsigned voff, unsigned soff) {
  __builtin_amdgcn_raw_buffer_store_b128(v, r, voff, soff, AUX);
  asm volatile("s_nop 1" :: "v"(v) : "memory");
}
DEVI void st128(u32x4 v, rsrc_t r, unsigned voff, unsigned soff) { st128a<16>(v, r, voff, soff); }
DEVI void st64(u32x2 v, rsrc_t r, unsigned voff, unsigned soff) { __builtin_amdgcn_raw_buffer_store_b64(v, r, voff, soff, 16); }

DEVI bool give_up(unsigned& spins) {
  asm volatile("s_sleep 2" ::: "memory");
  return ++spins > kSpinCap;
}
DEVI u32x2 next_tag(u32x2 g) {
  u64 v = ((u64)g.y << 32 | g.x) + 1;
  v += v == ~0ull;
  return (u32x2){(unsigned)v, (unsigned)(v >> 32)};
}
DEVI void wait_tag(rsrc_t rs, unsigned off, u32x2 tag) {
  for (unsigned spins = 0;;) {
    const u32x2 v = ld64(rs, off, 0);
    if (__builtin_amdgcn_ballot_w64(off != OOB && (v.x != tag.x || v.y != tag.y)) == 0 || give_up(spins)) break;
  }
  asm volatile("" ::: "memory");
}

DEVI void transpose8x4(const u32x2 (&r)[8], u32x4 (&o)[4]) {
#pragma unroll
  for (int p = 0; p < 4; ++p) {
    o[0][p] = __builtin_amdgcn_perm(r[2 * p + 1].x, r[2 * p].x, 0x05040100u);
    o[1][p] = __builtin_amdgcn_perm(r[2 * p + 1].x, r[2 * p].x, 0x07060302u);
    o[2][p] = __builtin_amdgcn_perm(r[2 * p + 1].y, r[2 * p].y, 0x05040100u);
    o[3][p] = __builtin_amdgcn_perm(r[2 * p + 1].y, r[2 * p].y, 0x07060302u);
  }
}

DEVI void wq_item_load(u32x2 (&r)[8], rsrc_t rw, int h, int kh, int qd, bool on) {
  const int mt = qd >> 3, rq = qd & 7;
  const int col = h * 20 + (rq < 5 ? mt * 640 + 4 * rq : 1280 + mt * 12 + 4 * (rq - 5));
  const unsigned off = (on && qd < 15) ? kh * 8 * 3840 + col * 2 : OOB;
#pragma unroll
  for (int j = 0; j < 8; ++j) r[j] = __builtin_amdgcn_raw_buffer_load_b64(rw, off, j * 3840, 0);
}
DEVI unsigned wq_item_pos(int kh, int qd) { return (kh >> 1) * 2048 + (qd >> 3) * 1024 + (kh & 1) * 512 + (qd & 7) * 64; }

struct Frag { u32x4 k1; u32x2 k2; u32x4 va, vb; };

template <bool QK, bool LDK, bool LDV>
DEVI void att_step(f32x16& cur, f32x16& oth, f32x16& o, Frag& f, const u32x4& q1, const u32x2& q2, const f32x16& cin,
                   unsigned a_vb, unsigned a_k1, unsigned a_k2, unsigned a_va) {
  o = pv_hi(f.vb, oth, o);
  f.vb = lds_r128(a_vb);
  SB();
#pragma unroll
  for (int i = 0; i < 4; ++i) cur[i] = __builtin_amdgcn_exp2f(cur[i]);
  SB();
  if (QK) oth = mfma32(f.k1, q1, cin);
  if (LDK) f.k1 = lds_r128(a_k1);
  SB();
#pragma unroll
  for (int i = 4; i < 8; ++i) cur[i] = __builtin_amdgcn_exp2f(cur[i]);
  SB();
  if (QK) oth = mfma32k8(f.k2, q2, oth);
  if (LDK) f.k2 = lds_r64(a_k2);
  SB();
#pragma unroll
  for (int i = 8; i < 12; ++i) cur[i] = __builtin_amdgcn_exp2f(cur[i]);
  SB();
  o = pv_lo(f.va, cur, o);
  if (LDV) f.va = lds_r128(a_va);
  SB();
#pragma unroll
  for (int i = 12; i < 16; ++i) cur[i] = __builtin_amdgcn_exp2f(cur[i]);
  SB();
}
DEVI void prio_if(int on) {
  asm volatile("s_cmp_eq_u32 %0, 0\n\ts_cbranch_scc1 .Lskip%=\n\ts_setprio 1\n.Lskip%=:" :: "s"(on) : "scc");
}
DEVI int opaque(int v) { asm volatile("" : "+v"(v)); return v; }

DEVI float max16(const f32x16& s) {
  float m = fmaxf(fmaxf(s[0], s[1]), s[2]);
#pragma unroll
  for (int i = 3; i < 15; i += 2) m = fmaxf(fmaxf(m, s[i]), s[i + 1]);
  return fmaxf(m, s[15]);
}
DEVI void set_shift(u32x2& q2, float m, int hh) {
  unsigned hi, mid, lo;
  split3(-m, hi, mid, lo);
  q2.x = hh ? (hi >> 16) | mid : q2.x;
  q2.y = hh ? (lo >> 16) | 0xFF7F0000u : q2.y;
}
template <bool DIAG> DEVI f32x16 tri_mask() {
  f32x16 c;
#pragma unroll
  for (int i = 0; i < 16; ++i) {
    const int key = (i & 3) + 8 * (i >> 2);
    const u64 above = ((1ull << key) - 1) | ((1ull << (key + 4)) - 1) << 32;
    c[i] = __builtin_amdgcn_inverse_ballot_w64(DIAG ? above : ~above) ? -3.0e38f : 0.f;
  }
  return c;
}

DEVI void ev_wait_if_eq(int a, int b, unsigned addr, unsigned target) {
  unsigned sv, cnt, v;
  asm volatile("s_cmp_lg_u32 %3, %4\n\ts_cbranch_scc1 .Lgo%=\n\ts_mov_b32 %1, 0x100000\n\ts_nop 7\n\ts_nop 3\n"
               ".Lspin%=:\n\tds_read_b32 %2, %5\n\ts_waitcnt lgkmcnt(0)\n\tv_readfirstlane_b32 %0, %2\n\t"
               "s_cmp_ge_u32 %0, %6\n\ts_cbranch_scc1 .Lgo%=\n\ts_sleep 1\n\ts_sub_u32 %1, %1, 1\n\ts_cbranch_scc0 .Lspin%=\n"
               ".Lgo%=:"
               : "=&s"(sv), "=&s"(cnt), "=&v"(v) : "s"(a), "s"(b), "v"(addr), "s"(target) : "memory", "scc");
}
DEVI void unit_gate(int n, unsigned evv) {
  unsigned sv, cnt, v, a;
  asm volatile("s_bitcmp1_b32 0x3200, %4\n\ts_cbranch_scc0 .Lgo%=\n\ts_nop 7\n\ts_nop 3\n\t"
               "v_lshl_add_u32 %3, %4, 2, %5\n\tv_mov_b32 %2, 1\n\ts_mov_b64 exec, 1\n\tds_add_u32 %3, %2 offset:%6\n\ts_mov_b64 exec, -1\n\t"
               "s_mov_b32 %1, 0x100000\n"
               ".Lspin%=:\n\tds_read_b32 %2, %3 offset:%7\n\ts_waitcnt lgkmcnt(0)\n\tv_readfirstlane_b32 %0, %2\n\t"
               "s_cmp_ge_u32 %0, 4\n\ts_cbranch_scc1 .Lgo%=\n\ts_sleep 1\n\ts_sub_u32 %1, %1, 1\n\ts_cbranch_scc0 .Lspin%=\n"
               ".Lgo%=:"
               : "=&s"(sv), "=&s"(cnt), "=&v"(v), "=&v"(a) : "s"(n), "v"(evv), "i"(C_DONE * 4), "i"(C_RDY * 4 + 4) : "memory", "scc");
}
DEVI void ev_post(unsigned addr, unsigned n = 1) {
  asm volatile("s_mov_b64 exec, 1\n\tds_add_u32 %0, %1\n\ts_mov_b64 exec, -1" :: "v"(addr), "v"(n) : "memory");
}
DEVI unsigned ev_peek(unsigned addr) { return *(volatile LAS unsigned*)addr; }
template <int NAP = 1> DEVI void ev_wait_from(unsigned seen, unsigned addr, unsigned target) {
  unsigned sv, cnt;
  asm volatile("s_nop 0\n\tv_readfirstlane_b32 %0, %2\n\ts_cmp_ge_u32 %0, %4\n\ts_cbranch_scc1 .Lgo%=\n\t"
               "s_mov_b32 %1, 0x100000\n"
               ".Lspin%=:\n\ts_sleep %5\n\tds_read_b32 %2, %3\n\ts_sub_u32 %1, %1, 1\n\ts_cbranch_scc1 .Lout%=\n\t"
               "s_waitcnt lgkmcnt(0)\n\tv_readfirstlane_b32 %0, %2\n\t"
               "s_cmp_ge_u32 %0, %4\n\ts_cbranch_scc0 .Lspin%=\n"
               ".Lout%=:\n\ts_waitcnt lgkmcnt(0)\n"
               ".Lgo%=:"
               : "=&s"(sv), "=&s"(cnt), "+&v"(seen)
               : "v"(addr), "s"(__builtin_amdgcn_readfirstlane(target)), "i"(NAP) : "memory", "scc");
}
template <int NAP = 1> DEVI void ev_wait(unsigned addr, unsigned target) { ev_wait_from<NAP>(ev_peek(addr), addr, target); }

DEVI void kv_publish(unsigned l0, unsigned l5, rsrc_t rs, unsigned un, unsigned slot) {
  u32x4 g[6];
#pragma unroll
  for (int i = 0; i < 6; ++i) g[i] = lds_r128(L_U + slot * UNIT + l0 + i * 2048);
#pragma unroll
  for (int i = 0; i < 6; ++i) st128(g[i], rs, i < 5 ? l0 : l5, OFF_KV + un * UNIT + i * 2048);
}
DEVI unsigned unit_home(int n) { return L_U + (n < 5 ? n : n - 13) * UNIT; }
DEVI void kv_fetch(unsigned l16, u32x4 srd, unsigned un, unsigned dst, int first, int step) {
  for (int i = first; i < 12; i += step) {
    const unsigned o = i < 11 ? i * 1024 : UNIT - 1024;
    dma1k(dst + o, l16, srd, OFF_KV + un * UNIT + o);
  }
}
DEVI int kv_order(int c, int j) { return c < 4 ? (j < 4 ? (c - j) & 3 : 4 + ((c - j) & 3)) : (j < 5 ? (c - j) & 3 : 4 + ((c - j) & 3)); }
DEVI void kv_fake(unsigned l0, unsigned l5, unsigned slot) {
#pragma unroll
  for (int i = 0; i < 6; ++i) {
    const unsigned o = (l0 + i * 2048) % TILE, one = (o >= 1280 && o < T_V) ? 0x3F800000u : 0u;
    if (i < 5 || l5 != OOB) lds_w128(L_U + slot * UNIT + l0 + i * 2048, (u32x4){0u, one, 0u, one});
  }
}
DEVI unsigned o_piece(unsigned q, int l, int c, int h) { return OFF_O + l * XIMG + c * 327680 + h * 10240 + q * 1024; }
DEVI unsigned x_piece(unsigned q, int l, int c, int h) { return OFF_X + l * XIM2 + c * 8 * XT + h * (XT / 4) + q * 1024; }
DEVI void add_colsum(const u32x4 (&wn)[20], int u, int lane, unsigned dst) {
  float s0 = 0.f, s1 = 0.f;
  const bf16x2 ones = {(__bf16)1.0f, (__bf16)1.0f};
#pragma unroll
  for (int i = 0; i < 20; ++i) {
    const unsigned a = wn[i].x, b = wn[i].y, c = wn[i].z, d = wn[i].w;
    s0 = __builtin_amdgcn_fdot2_f32_bf16(__builtin_bit_cast(bf16x2, a), ones, s0, false);
    s1 = __builtin_amdgcn_fdot2_f32_bf16(__builtin_bit_cast(bf16x2, b), ones, s1, false);
    s0 = __builtin_amdgcn_fdot2_f32_bf16(__builtin_bit_cast(bf16x2, c), ones, s0, false);
    s1 = __builtin_amdgcn_fdot2_f32_bf16(__builtin_bit_cast(bf16x2, d), ones, s1, false);
  }
  s0 += s1;
  s0 = half_lo(s0) + half_hi(s0);
  if (lane < 32) __hip_atomic_fetch_add((LAS float*)(dst + (u & 1) * 128 + lane * 4), s0, __ATOMIC_RELAXED, __HIP_MEMORY_SCOPE_WORKGROUP);
}

DEVI void weight_share(int task, int lane, int c, int h, const u16* wq, const u16* wo, rsrc_t rs, unsigned bounce, bool bar) {
  const int m2 = task >> 2, wi = task & 3;
  u32x2 r[8];
  u32x4 o[4];
  unsigned dst, pos, np;
  if (wi < 3) {
    const rsrc_t rw = __builtin_amdgcn_make_buffer_rsrc((void*)wq, 0, 640 * 3840, 0x00020000);
    wq_item_load(r, rw, h, c * 10 + wi * 4 + (lane >> 4), lane & 15, wi < 2 || lane < 32);
    pos = wq_item_pos(lane >> 4, lane & 15);
    dst = OFF_WQ + (m2 * 32 + h) * WQ_HEAD + (c * 10 + wi * 4) * 1024;
    np = wi < 2 ? 4 : 2;
  } else {
    const int b32 = (h >> 3) * 8 + c, p = b32 * 4 + (lane >> 4), nt = p % 5, q = (p / 5) * 4 + ((lane >> 2) & 3), g = lane & 3;
    const int c4 = nt < 4 ? (nt >> 1) * 8 + g * 2 + (nt & 1) : 16 + g;
    const rsrc_t rw = __builtin_amdgcn_make_buffer_rsrc((void*)wo, 0, 640 * 1280, 0x00020000);
    const int k0 = q < 64 ? (q >> 1) * 20 + (q & 1) * 4 : (q - 64) * 40 + 16, dk = q < 64 ? 8 : 20;
    const unsigned wof = b32 < 25 ? k0 * 1280 + ((h & 7) * 80 + c4 * 4) * 2 : OOB;
#pragma unroll
    for (int j = 0; j < 4; ++j) {
      r[j] = __builtin_amdgcn_raw_buffer_load_b64(rw, wof, j * 1280, 0);
      r[4 + j] = __builtin_amdgcn_raw_buffer_load_b64(rw, b32 < 25 ? wof + dk * 1280 : OOB, j * 1280, 0);
    }
    pos = lane * 64;
    dst = OFF_WO + (m2 * 8 + (h & 7)) * WO_CG + b32 * 4096;
    np = b32 < 25 ? 4 : 0;
  }
  if (bar) bar_lds();
  transpose8x4(r, o);
#pragma unroll
  for (int i = 0; i < 4; ++i) if (pos < np * 1024) lds_w128(bounce + pos + i * 16, o[i]);
#pragma unroll
  for (int i = 0; i < 4; ++i) o[i] = lds_r128(bounce + i * 1024 + lane * 16);
#pragma unroll
  for (int i = 0; i < 4; ++i) st128(o[i], rs, (unsigned)i < np ? lane * 16 : OOB, dst + i * 1024);
}

__global__ void __launch_bounds__(768, 3) main_kernel(const u16* __restrict__ x, const u16* __restrict__ wqA, const u16* __restrict__ woA,
                                                      const u16* __restrict__ wqB, const u16* __restrict__ woB,
                                                      char* __restrict__ ws, u16* __restrict__ out) {
  const int tid0 = threadIdx.x;
  const int w = __builtin_amdgcn_readfirstlane(tid0 >> 6);
  const int y = (w >> 2) & 1;
  const int tt = y ? 7 - w : w + 4;
  const int xg = blockIdx.x & 7, xi = blockIdx.x >> 3;
  const int c = ((xi & 1) << 2) | ((xg & 1) << 1) | ((xi >> 1) & 1);
  const int h = ((xg >> 1) << 3) | (xi >> 2);
#define LANE_VARS const int tid = opaque(tid0), lane = tid & 63, t = lane & 31, hh = lane >> 5; (void)t; (void)hh
  const rsrc_t rs = __builtin_amdgcn_make_buffer_rsrc(ws, 0, WS_USED, 0x00020000);
  const rsrc_t ro = __builtin_amdgcn_make_buffer_rsrc(out, 0, XIMG, 0x00020000);
  const u32x4 srd = {(unsigned)(size_t)ws, (unsigned)((size_t)ws >> 32) & 0xffffu, WS_USED, 0x00020000u};
  const f32x16 zero16 = {0.f, 0.f, 0.f, 0.f, 0.f, 0.f, 0.f, 0.f, 0.f, 0.f, 0.f, 0.f, 0.f, 0.f, 0.f, 0.f};
  if (tid0 < 384) *(LAS unsigned*)(L_M + tid0 * 4) = 0u;

  if (w >= 8) {
    const int u = w - 8, lane = tid0 & 63, me = h * 8 + c;
    const unsigned l16 = lane * 16, l4 = u * 1024 + l16;
    const unsigned l0 = (u & 1) * 1024 + l16, l5 = l0 + 10240 < UNIT ? l0 : OOB;
    const u32x4 sv = {SENT, SENT, SENT, SENT};
    __builtin_amdgcn_s_setprio(1);
    u32x2 tag = ld64(rs, 0, OFF_FLAG);
    if (u == 0) {
      const rsrc_t rx = __builtin_amdgcn_make_buffer_rsrc((void*)x, 0, XIMG, 0x00020000);
      const int row = c * 256 + h * 8 + (lane & 7), p = lane >> 3;
      u32x4 v[10];
#pragma unroll
      for (int i = 0; i < 10; ++i) v[i] = __builtin_amdgcn_raw_buffer_load_b128(rx, row * 1280 + p * 16, i * 128, 0);
      st128(sv, rs, lane < 16 ? OFF_F1 + p * 32768 + me * 128 + (lane & 7) * 16 : OOB, 0);
      st128(sv, rs, OFF_KF + (((p >> 1) * 32 + h) * 16 + 2 * c + (p & 1)) * 128 + (lane & 7) * 16, 0);
      bar_lds();
      const unsigned xd = OFF_X + (row >> 5) * XT;
      float ss = 0.f, sm = 0.f;
#pragma unroll
      for (int i = 0; i < 10; ++i) {
        st128(clean4(v[i]), rs, xd + p * 512 + (row & 31) * 16, i * 4096);
        ss = sq2(v[i].x, ss); ss = sq2(v[i].y, ss); ss = sq2(v[i].z, ss); ss = sq2(v[i].w, ss);
        sm = sum2(v[i].x, sm); sm = sum2(v[i].y, sm); sm = sum2(v[i].z, sm); sm = sum2(v[i].w, sm);
      }
      st128(clean4((u32x4){__float_as_uint(sm), __float_as_uint(ss), 0u, 0u}), rs, xd + 40960 + (row & 31) * 16 + p * 512, 0);
      tag = next_tag(tag);
      drain_vm();
      ev_wait(L_C + C_W0 * 4, 3);
      st64(tag, rs, lane == 0 ? 0u : OOB, OFF_F1 + me * 128);
      ev_post(L_C + C_F1 * 4);
      wait_tag(rs, lane < 40 ? OFF_F1 + (lane < 32 ? lane * 8 + c : h * 8 + lane - 32) * 128 : OOB, tag);
      ev_post(L_C + C_X0 * 4);
    } else {
      bar_lds();
      tag = next_tag(tag);
      ev_wait(L_C + C_X0 * 4, 1);
    }
    {
      u32x4 wn[20];
#pragma unroll
      for (int i = 0; i < 20; ++i) wn[i] = ld128(rs, l4, OFF_WQ + h * WQ_HEAD + i * 4096);
#pragma unroll
      for (int q = 0; q < 4; ++q) {
#pragma unroll
        for (int i = 5 * q; i < 5 * q + 5; ++i) lds_w128(L_A + l4 + i * 4096, wn[i]);
        ev_post(L_C + (C_WQ + q) * 4);
      }
      if (u) {
        for (unsigned q = 0; q < 10u + (u == 3); ++q) {
          st128(sv, rs, l16, u < 3 ? o_piece(q, u - 1, c, h) : x_piece(q, 1, c, h));
          __builtin_amdgcn_s_sleep(2);
        }
      }
      add_colsum(wn, u, lane, L_M);
      ev_post(L_C + C_CS * 4);
      drain_vm();
      ev_post(L_C + C_INIT * 4);
      if (u == 3) {
        ev_wait<4>(L_C + C_INIT * 4, 12);
        st64(tag, rs, lane == 0 ? 0u : OOB, OFF_F2 + me * 128);
      }
    }
    for (int layer = 0; layer < 4; ++layer) {
      const int m = layer & 1;
      const unsigned ev = L_C + layer * 256;
      const unsigned ub = (layer * 32 + h) * 16, wo_src = OFF_WO + (m * 8 + (h & 7)) * WO_CG;
      const unsigned hi = u < 2;
      const unsigned own = m ? 1 - hi : 3 + hi;
      const unsigned wq_next = layer < 3 ? OFF_WQ + ((m ^ 1) * 32 + h) * WQ_HEAD : OOB;
      ev_wait<8>(ev + C_PR * 4, 1);
      if (!hi && layer == 2) {
        ev_wait<2>(ev + C_PR * 4, 8);
        for (int p = u - 2; p < WOE; p += 2) dma1k(L_A + p * 1024, l16, srd, wo_src + p * 1024);
      }
      ev_wait(ev + (C_RDY + own) * 4, 4);
      kv_publish(l0, l5, rs, ub + 2 * c + hi, own);
      drain_vm();
      st64(tag, rs, lane == 0 ? (u & 1) * 8 : OOB, OFF_KF + (ub + 2 * c + hi) * 128);
      ev_post(ev + C_PUB * 4);
      {
        const unsigned un = ub + 2 * (m ? kv_order(c, 1) : (c - 1) & 7) + hi, slot = m ? 3 - hi : 1 + hi;
        ev_wait(ev + C_PR * 4, 8);
        if (m || c > 0) {
          wait_tag(rs, lane < 2 ? OFF_KF + un * 128 + lane * 8 : OOB, tag);
          kv_fetch(l16, srd, un, L_U + slot * UNIT, u & 1, 2);
          drain_vm();
        } else {
          kv_fake(l0, l5, slot);
        }
        ev_post(ev + (C_RDY + slot) * 4, 2);
      }
      if (m == 0) {
        if (layer == 0) {
          wait_tag(rs, OFF_F2 + (lane < 32 ? lane * 8 + c : (((lane >> 3) & 3) * 8 + (h & 7)) * 8 + (lane & 7)) * 128, tag);
          ev_post(L_C + C_SEEN * 4);
          ev_wait(ev + C_PUB * 4, 4);
          for (int p = u; p < 100; p += 4) dma1k(L_A + p * 1024, l16, srd, wo_src + p * 1024);
        } else if (hi) {
          ev_wait(ev + (C_RDY + 3) * 4, 4);
          for (int p = WOE + u; p < 100; p += 2) dma1k(L_A + p * 1024, l16, srd, wo_src + p * 1024);
        }
      } else {
        ev_wait(ev + C_PUB * 4, 4);
        ev_post(ev + (C_RDY + 10) * 4); ev_post(ev + (C_RDY + 14) * 4);
        for (int n = 4 + u; n < 17; n += 4) {
          if (n >= 13 && layer == 1) {
            for (unsigned q = 0; q < 10u + (u >> 1); ++q) {
              st128(sv, rs, l16, u < 2 ? o_piece(q, 2 + u, c, h) : x_piece(q, u, c, h));
              __builtin_amdgcn_s_sleep(2);
            }
          }
          if (n < 16) {
            const unsigned un2 = ub + 2 * kv_order(c, n >> 1) + 1 - (n & 1);
            wait_tag(rs, lane < 2 ? OFF_KF + un2 * 128 + lane * 8 : OOB, tag);
            if (n >= 13) ev_wait<8>(ev + (C_DONE + 3) * 4, 8);
            kv_fetch(l16, srd, un2, unit_home(n), 0, 1);
          }
          drain_vm();
          if (n != 8) ev_post(ev + (C_RDY + (n < 8 ? n : n < 13 ? 8 : 13)) * 4, n < 8 ? 4 : 1);
        }
        if (layer == 3) {
          ev_wait<4>(ev + (C_RDY + 13) * 4, 4);
          st64(tag, rs, (tid0 == 512 && blockIdx.x == 0) ? 0u : OOB, OFF_FLAG);
        }
      }
      if (m == 0) {
        drain_vm();
        ev_post(ev + C_WO * 4);
        ev_wait<4>(ev + C_OS * 4, 8);
        ev_wait<2>(ev + C_OL * 4, 4);
      }
      u32x4 wn[20];
#pragma unroll
      for (int i = 0; i < 20; ++i) wn[i] = ld128(rs, l4, wq_next + i * 4096);
      SB();
      if (m) {
        ev_wait<16>(ev + (C_DONE + 9) * 4, 8);
        for (int p = u; p < 52; p += 4) { dma1k(L_A + p * 1024, l16, srd, wo_src + p * 1024); __builtin_amdgcn_s_sleep(1); }
        ev_wait<8>(ev + (C_DONE + 13) * 4, 8);
        for (int p = 52 + u; p < 100; p += 4) dma1k(L_A + p * 1024, l16, srd, wo_src + p * 1024);
        drain_vm();
        ev_post(ev + C_WO * 4);
        if (layer == 3) break;
      }
      if (m) { ev_wait<8>(ev + C_OS * 4, 1); ev_wait<4>(ev + C_OL * 4, 4); }
      ev_wait(ev + C_OP * 4, 8);
#pragma unroll
      for (int q = 0; q < 4; ++q) {
#pragma unroll
        for (int i = 5 * q; i < 5 * q + 5; ++i) lds_w128(L_A + l4 + i * 4096, wn[i]);
        ev_post(ev + 256 + (C_WQ + q) * 4);
      }
      if (layer == 0) { add_colsum(wn, u, lane, L_M + 256); ev_post(ev + 256 + C_CS * 4); }
    }
    return;
  }

  {
    const int lane = tid0 & 63;
    if (w < 3) {
      weight_share(w, lane, c, h, wqA, woA, rs, L_U + w * 4096, true);
      drain_vm();
      ev_post(L_C + C_W0 * 4);
    } else {
      const unsigned id = (blockIdx.x * 5 + w - 3) * 64 + lane;
      const rsrc_t r1 = __builtin_amdgcn_make_buffer_rsrc((void*)wqB, 0, 640 * 3840, 0x00020000);
      const rsrc_t r2 = __builtin_amdgcn_make_buffer_rsrc((void*)woA, 0, 640 * 1280, 0x00020000);
      const rsrc_t r3 = __builtin_amdgcn_make_buffer_rsrc((void*)woB, 0, 640 * 1280, 0x00020000);
      const u32x4 p0 = __builtin_amdgcn_raw_buffer_load_b128(r1, id * 16, 0, 0), p1 = __builtin_amdgcn_raw_buffer_load_b128(r1, id * 16, 1310720, 0);
      const u32x4 p2 = __builtin_amdgcn_raw_buffer_load_b128(r2, id * 16, 0, 0), p3 = __builtin_amdgcn_raw_buffer_load_b128(r3, id * 16, 0, 0);
      bar_lds();
      asm volatile("" :: "v"(p0), "v"(p1), "v"(p2), "v"(p3));
      ev_wait(L_C + C_F1 * 4, 1);
      weight_share(w, lane, c, h, wqB, w == 3 ? woA : woB, rs, L_U + w * 4096, false);
    }
    ev_wait(L_C + C_X0 * 4, 1);
    __builtin_amdgcn_s_sleep(10);
  }

  for (int layer = 0; layer < 4; ++layer) {
    const int m = layer & 1;
    const unsigned ev = L_C + layer * 256;
    u32x4 q1, k1; u32x2 q2, k2;
    {
      LANE_VARS;
      __builtin_amdgcn_s_setprio(1);
      const unsigned xoff = OFF_X + layer * XIM2 + (c * 8 + tt) * XT;
      const unsigned xl = lane * 16;
#define XLD(k) ld128(rs, xl + ((k) & 3) * 1024, xoff + ((k) >> 2) * 4096)
#define XISS(ks) (XI0 + 2 * (ks) < XD + (ks) ? XI0 + 2 * (ks) : XD + (ks))
      u32x4 xb[40];
      u32x4 sq[4];
#pragma unroll
      for (int ks = 0; ks < XI0; ++ks) xb[ks] = XLD(ks);
      SB();
      const unsigned wa = opaque(L_A + lane * 16), wb = opaque(L_A + 40960 + lane * 16);
      u32x4 wf[3][2];
      ev_wait(ev + C_WQ * 4, 4);
#pragma unroll
      for (int ks = 0; ks < 2; ++ks) { wf[ks][0] = lds_r128(wa + ks * 2048); wf[ks][1] = lds_r128(wa + ks * 2048 + 1024); }
      f32x16 a0 = zero16, a1 = zero16;
      unsigned seen = 0;
      float rstd = 0.f;
      u32x4 cb, ca0, ca1;
#define SQLD(i) ld128(rs, t * 16 + hh * 2048 + (i) * 512, xoff + 40960)
#define SQMAX umax4(umax4(umax4(umax4(0u, sq[0]), sq[1]), sq[2]), sq[3])
#pragma unroll
      for (int g = 0; g < 8; ++g) {
        unsigned mx = 0;
#pragma unroll
        for (int i = 0; i < 5; ++i) mx = umax4(mx, xb[5 * g + i]);
        if (g == 7) mx = umax(mx, SQMAX);
        if (__builtin_expect(unwritten(mx), 0)) {
          unsigned spins = 0;
          do {
            if (give_up(spins)) break;
#pragma unroll
            for (int i = 0; i < 5; ++i) xb[5 * g + i] = XLD(5 * g + i);
            mx = 0;
            if (g == 7) {
#pragma unroll
              for (int i = 0; i < 4; ++i) sq[i] = SQLD(i);
              mx = SQMAX;
            }
#pragma unroll
            for (int i = 0; i < 5; ++i) mx = umax4(mx, xb[5 * g + i]);
          } while (unwritten(mx));
#pragma unroll
          for (int i = 5 * g + 5; i < 40; ++i) if (i < XISS(5 * g)) xb[i] = XLD(i);
        }
        if (g == 7) { drain_vm(); ev_post(ev + C_INIT * 4); }
#pragma unroll
        for (int i = 0; i < 5; ++i) {
          const int ks = 5 * g + i, kn = ks + 2, kx = XISS(ks), ky = XISS(ks + 1) - 1;
          if (kn < 40 && kn % 10 == 8) seen = ev_peek(kn < 38 ? ev + (C_WQ + (kn + 2) / 10) * 4 : L_C);
          if (kn < 40 && kn % 10 == 0) ev_wait_from(seen, ev + (C_WQ + kn / 10) * 4, 4);
          if (kn == 32) seen = ev_peek(L_C + m * 256 + C_CS * 4);
          if (kn == 34) {
            ev_wait_from(seen, L_C + m * 256 + C_CS * 4, 4);
            unsigned h0, m0, l0, h1, m1, l1;
            split3(*(LAS float*)(L_M + m * 256 + t * 4), h0, m0, l0);
            split3(*(LAS float*)(L_M + m * 256 + 128 + t * 4), h1, m1, l1);
            ca0 = (u32x4){(h0 >> 16) | h0, (m0 >> 16) | m0, (h0 >> 16) | l0, 0u};
            ca1 = (u32x4){(h1 >> 16) | h1, (m1 >> 16) | m1, (h1 >> 16) | l1, 0u};
          }
          if (kn < 40) {
            const unsigned ad = kn < 20 ? wa + kn * 2048 : wb + (kn - 20) * 2048;
            wf[kn % 3][0] = lds_r128(ad); wf[kn % 3][1] = lds_r128(ad + 1024);
          }
          SB();
          a0 = mfma32(wf[ks % 3][0], xb[ks], a0);
          if (ky > kx && kx < 40) xb[kx] = XLD(kx);
          if (ks == 36) {
            const f32x4 st = (__builtin_bit_cast(f32x4, sq[0]) + __builtin_bit_cast(f32x4, sq[1])) + (__builtin_bit_cast(f32x4, sq[2]) + __builtin_bit_cast(f32x4, sq[3]));
            float sm = st[0] + st[2], ss = st[1] + st[3];
            sm = half_lo(sm) + half_hi(sm);
            ss = half_lo(ss) + half_hi(ss);
            const float mean = sm * (1.f / 640.f);
            rstd = __builtin_amdgcn_rsqf(fmaxf(ss * (1.f / 640.f) - mean * mean, 0.f) + 1e-5f);
            unsigned mh, mm, ml;
            split3(hh ? 0.f : -mean, mh, mm, ml);
            cb = (u32x4){(mh >> 16) | mm, (mh >> 16) | mm, (ml >> 16) | mh, 0u};
          }
          SB();
          a1 = mfma32(wf[ks % 3][1], xb[ks], a1);
          if (ky < 40) xb[ky] = XLD(ky);
          if (ky >= 40 && ky < 44) sq[ky - 40] = SQLD(ky - 40);
          if (ks == 28) ev_post(ev + C_PR * 4);
          SB();
        }
      }
      a1 = mfma32(ca1, cb, a1);
      a0 = mfma32(ca0, cb, a0);
      const unsigned own = m ? y : 4 - y;
      const unsigned tb = L_U + own * UNIT + (tt & 3) * TILE;
      const unsigned vb = tb + T_V + (t >> 3) * VOCT + (t & 7) * 2;
      const unsigned vA = vb + hh * 64, vB = vb + (hh ? 14 : 8) * 16, vC = vb + (hh ? 18 : 12) * 16;
      const float rq = rstd * (0.22360679774997896f * 1.4426950408889634f), r2 = hh ? rstd : rq;
      SB();
      const unsigned c0 = pack2(a1[8] * rstd, a1[9] * rstd), c1 = pack2(a1[10] * rstd, a1[11] * rstd);
      const unsigned d0 = pack2(a1[12] * rstd, a1[13] * rstd), d1 = pack2(a1[14] * rstd, a1[15] * rstd);
      k1 = (u32x4){pack2(a1[0] * rstd, a1[1] * rstd), pack2(a1[2] * rstd, a1[3] * rstd), pack2(a1[4] * rstd, a1[5] * rstd), pack2(a1[6] * rstd, a1[7] * rstd)};
      k2 = (u32x2){hh ? 0x3F803F80u : c0, hh ? 0x00003F80u : c1};
      lds_w128(tb + lane * 16, k1);
      lds_w64(tb + T_K2 + lane * 8, k2);
      const unsigned cc = hh ? c0 : 0x3F800000u;
      lds_w16lo(vC, cc); lds_w16hi(vC + 16, cc); lds_w16lo(vC + 32, c1); lds_w16hi(vC + 48, c1);
      const unsigned e0 = pack2(a0[8] * r2, a0[9] * r2), e1 = pack2(a0[10] * r2, a0[11] * r2);
      const unsigned f0 = pack2(a0[12] * rstd, a0[13] * rstd), f1 = pack2(a0[14] * rstd, a0[15] * rstd);
      const unsigned b0 = hh ? e0 : d0, b1 = hh ? e1 : d1;
      asm volatile("" ::: "memory");
      lds_w16lo(vB, b0); lds_w16hi(vB + 16, b0); lds_w16lo(vB + 32, b1); lds_w16hi(vB + 48, b1);
      lds_w16lo(vA, f0); lds_w16hi(vA + 16, f0); lds_w16lo(vA + 32, f1); lds_w16hi(vA + 48, f1);
      ev_post(ev + (C_RDY + own) * 4);
      q1 = (u32x4){pack2(a0[0] * rq, a0[1] * rq), pack2(a0[2] * rq, a0[3] * rq), pack2(a0[4] * rq, a0[5] * rq), pack2(a0[6] * rq, a0[7] * rq)};
      q2 = (u32x2){hh ? 0u : e0, hh ? 0u : e1};
      __builtin_amdgcn_s_setprio(0);
    }

    {
      LANE_VARS;
      const unsigned kvl = OFF_KV + layer * KV_LAYER + h * 16 * UNIT;
      const unsigned vl = T_V + hh * VOCT + vpos(t) * 16;
      const unsigned lk = L_U + lane * 16, lk2 = L_U + T_K2 + lane * 8, lv = L_U + vl;
      f32x16 o = zero16, sa, sb = zero16;
      Frag f;
      f.vb = (u32x4){0u, 0u, 0u, 0u};
      float mx;
      if (m == 1) {
        sa = mfma32k8(k2, q2, mfma32(k1, q1, zero16));
        SB();
        ev_wait(ev + C_RDY * 4, 4);
        const u32x4 g1 = lds_r128(lk);
        const u32x2 g2 = lds_r64(lk2);
        f.va = lds_r128(lv);
        f.k1 = lds_r128(lk + TILE);
        f.k2 = lds_r64(lk2 + TILE);
        SB();
        mx = max16(sa);
        mx = fmaxf(half_lo(mx), half_hi(mx));
        set_shift(q2, mx, hh);
        sa = mfma32k8(g2, q2, mfma32(g1, q1, zero16));
        unsigned kb = lk, k2b = lk2, vc = lv, nxt = 0;
        const unsigned evv = opaque(ev);
#define UNIT_BODY(PEEK, GATE) \
          prio_if(y); \
          att_step<1, 1, 1>(sa, sb, o, f, q1, q2, zero16, vc + 2 * VOCT, kb + 2 * TILE, k2b + 2 * TILE, vc + TILE); \
          PEEK; \
          att_step<1, 1, 1>(sb, sa, o, f, q1, q2, zero16, vc + TILE + 2 * VOCT, kb + 3 * TILE, k2b + 3 * TILE, vc + 2 * TILE); \
          GATE; \
          kb = lk + nxt; k2b = lk2 + nxt; \
          const unsigned vn = lv + nxt; \
          __builtin_amdgcn_s_setprio(0); \
          att_step<1, 1, 1>(sa, sb, o, f, q1, q2, zero16, vc + 2 * TILE + 2 * VOCT, kb, k2b, vc + 3 * TILE); \
          att_step<1, 1, 1>(sb, sa, o, f, q1, q2, zero16, vc + 3 * TILE + 2 * VOCT, kb + TILE, k2b + TILE, vn); \
          vc = vn;
        for (int n = 0; n < 8; ++n) {
          nxt += n == 4 ? UNIT - 13 * UNIT : UNIT;
          UNIT_BODY(const unsigned seen = ev_peek(ev + (C_RDY + n + 1) * 4), ev_wait_from<2>(seen, ev + (C_RDY + n + 1) * 4, 4))
        }
        ev_post(ev + (C_DONE + 3) * 4);
        for (int n = 8; n < 16; ++n) {
          nxt += UNIT;
          UNIT_BODY(, unit_gate(n, evv))
        }
        o = pv_hi(f.vb, sb, o);
      } else {
        sa = mfma32k8(k2, q2, mfma32(k1, q1, tri_mask<true>()));
        const f32x16 far = tri_mask<false>();
        const unsigned ka = lk + (4 + tt) * TILE, k2a = lk2 + (4 + tt) * TILE, va = lv + (4 + tt) * TILE;
        f.va = lds_r128(va + 8 * TILE);
        const unsigned sn = ev_peek(ev + (C_RDY + 4 - y) * 4);
        SB();
        mx = max16(sa);
        mx = fmaxf(half_lo(mx), half_hi(mx));
        set_shift(q2, mx, hh);
#pragma unroll
        for (int i = 0; i < 16; ++i) sa[i] -= mx;
        SB();
        ev_wait_from(sn, ev + (C_RDY + 4 - y) * 4, 4);
        const int x1 = tt & 3;
        const unsigned e1 = ev + (C_RDY + 3 - y) * 4, e2 = e1 - 4;
        ev_wait_if_eq(x1, 0, e1, 4);
        f.k1 = lds_r128(ka + 7 * TILE);
        f.k2 = lds_r64(k2a + 7 * TILE);
#define STEP_W(n, A, B) ev_wait_if_eq(x1, ((n) + 1) & 3, (n) < 3 ? e1 : e2, 4); \
        att_step<1, 1, 1>(A, B, o, f, q1, q2, zero16, va + (8 - (n)) * TILE + 2 * VOCT, ka + (6 - (n)) * TILE, k2a + (6 - (n)) * TILE, va + (7 - (n)) * TILE)
        STEP_W(0, sa, sb);
        STEP_W(1, sb, sa);
        STEP_W(2, sa, sb);
        STEP_W(3, sb, sa);
        STEP_W(4, sa, sb);
        STEP_W(5, sb, sa);
        STEP_W(6, sa, sb);
        att_step<1, 0, 1>(sb, sa, o, f, q1, q2, far, va + TILE + 2 * VOCT, 0, 0, va);
        att_step<0, 0, 0>(sa, sb, o, f, q1, q2, zero16, va + 2 * VOCT, 0, 0, 0);
        o = pv_hi(f.vb, sa, o);
      }
      __builtin_amdgcn_s_setprio(2);
      const float tot = ((o[0] + o[1]) + (o[2] + o[3])) + ((o[4] + o[5]) + (o[6] + o[7])) + ((o[8] + o[9]) + (o[10] + o[11]));
      const bool bad = !(fabsf(tot) <= 3.0e38f) | !(o[8] <= (hh ? SUM_LIMIT : 3.0e38f));
      if (__builtin_expect(__builtin_amdgcn_ballot_w64(bad) != 0, 0)) {
        const int qt = c * 8 + tt;
        const int lo = m ? 0 : (qt >= 8 ? qt - 8 : 0), hi = m ? 63 : qt;
        ev_wait(ev + C_PUB * 4, 4);
        if (hh) { q2.x = 0u; q2.y = 0u; }
        mx = -3.0e38f;
        for (int pass = 0; pass < 2; ++pass) {
          o = zero16;
          for (int kt = lo; kt <= hi; ++kt) {
            const unsigned so = kvl + kt * TILE;
            const u32x4 j1 = ld128(rs, lane * 16, so), v0 = ld128(rs, vl, so), v1 = ld128(rs, vl + 2 * VOCT, so);
            const u32x2 j2 = ld64(rs, lane * 8 + T_K2, so);
            f32x16 cin = zero16;
            if (!m && kt == qt) cin = tri_mask<true>();
            if (!m && kt == qt - 8) cin = tri_mask<false>();
            f32x16 s = mfma32k8(j2, q2, mfma32(j1, q1, cin));
            if (pass == 0) {
              mx = fmaxf(mx, max16(s));
            } else {
#pragma unroll
              for (int e = 0; e < 16; ++e) s[e] = __builtin_amdgcn_exp2f(s[e]);
              o = pv_lo(v0, s, o);
              o = pv_hi(v1, s, o);
            }
          }
          if (pass == 0) { mx = fmaxf(half_lo(mx), half_hi(mx)); set_shift(q2, mx, hh); }
        }
      }
      const float inv = __builtin_amdgcn_rcpf(half_hi(o[8]));
      const u32x4 p = {pack2(o[0] * inv, o[1] * inv), pack2(o[2] * inv, o[3] * inv), pack2(o[4] * inv, o[5] * inv), pack2(o[6] * inv, o[7] * inv)};
      const unsigned oo = OFF_O + layer * XIMG + (c * 16 + tt * 2) * 20480;
      const unsigned lo = (t >> 4) * 20480 + (t & 15) * 16;
      ev_wait_if_eq(layer, 0, L_C + C_SEEN * 4, 1);
      st128(clean4(p), rs, lo + hh * 256, oo + h * 512);
      st64(clean2((u32x2){pack2(o[8] * inv, o[9] * inv), pack2(o[10] * inv, o[11] * inv)}), rs, hh ? OOB : lo, oo + (64 + (h >> 1)) * 256 + (h & 1) * 8);
    }

    {
      LANE_VARS;
      const int cg = h & 7, t16 = lane & 15, g4 = lane >> 4, kh = (w >> 1) & 1;
      const int rt = c * 16 + ((h >> 3) + 4 - 4 * y) * 2 + (w & 1);
      const int row = rt * 16 + t16;
      const unsigned xl = (row >> 5) * XT + (row & 31) * 16 + cg * 5120;
      const unsigned xo = OFF_X + layer * XIM2;
      const unsigned xa = xl + (4 * kh + g4) * 512, xc = kh ? OOB : xl + (8 + (g4 >> 1)) * 512 + (g4 & 1) * 8;
      const u32x4 ra = ld128(rs, xa, xo);
      const u32x2 rc = ld64(rs, xc, xo);
      const unsigned oo = OFF_O + layer * XIMG + rt * 20480 + kh * 10240;
      const unsigned ol = lane * 16;
#define OLD(k) ld128(rs, ol + ((k) & 3) * 1024, oo + ((k) >> 2) * 4096)
      u32x4 ob[10];
      const unsigned wa0 = opaque(L_A + kh * 51200 + lane * 16);
      constexpr int WD = OWD;
      u32x4 wf[WD + 1];
      ev_post(ev + C_OS * 4);
      f32x4 acc[5];
#pragma unroll
      for (int n = 0; n < 5; ++n) acc[n] = (f32x4){0.f, 0.f, 0.f, 0.f};
      ev_wait(ev + C_OS * 4, 8);
      __builtin_amdgcn_s_sleep(6);
#pragma unroll
      for (int ks = 0; ks < 10; ++ks) ob[ks] = OLD(ks);
      SB();
      ev_wait(ev + C_WO * 4, 4);
#pragma unroll
      for (int i = 0; i < WD; ++i) wf[i] = lds_r128(wa0 + i * 1024);
#pragma unroll
      for (int g = 0; g < 2; ++g) {
        unsigned mx = 0;
#pragma unroll
        for (int i = 0; i < 5; ++i) mx = umax4(mx, ob[5 * g + i]);
        if (__builtin_expect(unwritten(mx), 0)) {
          unsigned spins = 0;
          do {
            if (give_up(spins)) break;
#pragma unroll
            for (int i = 5 * g; i < 10; ++i) ob[i] = OLD(i);
            mx = 0;
#pragma unroll
            for (int i = 0; i < 5; ++i) mx = umax4(mx, ob[5 * g + i]);
          } while (unwritten(mx));
        }
        if (g == 0) ev_post(ev + C_OL * 4);
#pragma unroll
        for (int e = 0; e < 25; ++e) {
          const int i = 25 * g + e, in = i + WD;
          if (in < 50) wf[in % (WD + 1)] = lds_r128(wa0 + in * 1024);
          acc[i % 5] = mfma16(wf[i % (WD + 1)], ob[i / 5], acc[i % 5]);
          SB();
        }
      }
      const unsigned sc = L_U + (m + y) * UNIT + (w & 1) * 5120 + lane * 16;
      const unsigned pe = ev + (C_RED + y * 2 + (w & 1)) * 4;
      f32x4 u0, u1;
      if (kh) {
        *(LAS f32x4*)(sc) = acc[0]; *(LAS f32x4*)(sc + 1024) = acc[1]; *(LAS f32x4*)(sc + 4096) = acc[4];
        u0 = acc[2]; u1 = acc[3];
      } else {
        *(LAS f32x4*)(sc + 2048) = acc[2]; *(LAS f32x4*)(sc + 3072) = acc[3];
        u0 = acc[0]; u1 = acc[1];
      }
      ev_post(pe);
      u0 += (f32x4){bf_lo(ra.x), bf_hi(ra.x), bf_lo(ra.y), bf_hi(ra.y)};
      u1 += (f32x4){bf_lo(ra.z), bf_hi(ra.z), bf_lo(ra.w), bf_hi(ra.w)};
      f32x4 u2 = acc[4] + (f32x4){bf_lo(rc.x), bf_hi(rc.x), bf_lo(rc.y), bf_hi(rc.y)};
      ev_wait<0>(pe, 2);
      const f32x4 p0 = *(LAS f32x4*)(sc + kh * 2048), p1 = *(LAS f32x4*)(sc + kh * 2048 + 1024), p2 = *(LAS f32x4*)(sc + 4096);
      SB();
      u0 += p0; u1 += p1; u2 += p2;
      const u32x4 na = {pack2(u0[0], u0[1]), pack2(u0[2], u0[3]), pack2(u1[0], u1[1]), pack2(u1[2], u1[3])};
      u32x2 nc = {pack2(u2[0], u2[1]), pack2(u2[2], u2[3])};
      if (layer < 3) {
        SB();
        st128(clean4(na), rs, xa, xo + XIM2);
        st64(clean2(nc), rs, xc, xo + XIM2);
        ev_post(ev + C_OP * 4);
        SB();
        if (kh) nc = (u32x2){0u, 0u};
        float s0 = sq2(na.x, 0.f), s1 = sum2(na.x, 0.f);
        s0 = sq2(na.y, s0); s1 = sum2(na.y, s1); s0 = sq2(na.z, s0); s1 = sum2(na.z, s1); s0 = sq2(na.w, s0); s1 = sum2(na.w, s1);
        s0 = sq2(nc.x, s0); s1 = sum2(nc.x, s1); s0 = sq2(nc.y, s0); s1 = sum2(nc.y, s1);
        s0 = half_lo(s0) + half_hi(s0);
        s1 = half_lo(s1) + half_hi(s1);
        const auto q0 = __builtin_amdgcn_permlane16_swap(__float_as_uint(s0), __float_as_uint(s0), false, false);
        const auto q1 = __builtin_amdgcn_permlane16_swap(__float_as_uint(s1), __float_as_uint(s1), false, false);
        s0 = __uint_as_float(q0[0]) + __uint_as_float(q0[1]);
        s1 = __uint_as_float(q1[0]) + __uint_as_float(q1[1]);
        st64(clean2((u32x2){__float_as_uint(s1), __float_as_uint(s0)}), rs,
             lane < 16 ? (row >> 5) * XT + 40960 + (row & 31) * 16 + cg * 512 + kh * 8 : OOB, xo + XIM2);
      } else {
        const unsigned od = row * 1280 + cg * 160;
        st128a<0>(na, ro, od + kh * 64 + g4 * 16, 0);
        __builtin_amdgcn_raw_buffer_store_b64(nc, ro, kh ? OOB : od + 128 + g4 * 8, 0, 0);
      }
      if (layer < 3) { ev_wait(ev + C_OP * 4, 8); __builtin_amdgcn_s_sleep(4); }
    }
  }
}

extern "C" void kernel_launch(void* const* d_in, const int* in_sizes, int n_in, void* d_out, int out_size, void* d_ws, size_t ws_size,
                              hipStream_t stream) {
  (void)in_sizes; (void)n_in; (void)out_size; (void)ws_size;
  (void)hipFuncSetAttribute((const void*)main_kernel, hipFuncAttributeMaxDynamicSharedMemorySize, LDS_BYTES);
  main_kernel<<<256, 768, LDS_BYTES, stream>>>((const u16*)d_in[0], (const u16*)d_in[1], (const u16*)d_in[2], (const u16*)d_in[4],
                                               (const u16*)d_in[5], (char*)d_ws, (u16*)d_out);
}
```

```cpp
#include <hip/hip_runtime.h>
#include <hip/hip_bf16.h>

typedef float f32x16 __attribute__((ext_vector_type(16)));
typedef float f32x8 __attribute__((ext_vector_type(8)));
typedef float f32x4 __attribute__((ext_vector_type(4)));
typedef unsigned u32x4 __attribute__((ext_vector_type(4)));
typedef unsigned u32x2 __attribute__((ext_vector_type(2)));
typedef __bf16 bf16x16 __attribute__((ext_vector_type(16)));
typedef __bf16 bf16x8 __attribute__((ext_vector_type(8)));
typedef __bf16 bf16x2 __attribute__((ext_vector_type(2)));
typedef short s16x4 __attribute__((ext_vector_type(4)));
typedef unsigned short u16;

#define LAS __attribute__((address_space(3)))
#define GAS __attribute__((address_space(1)))
typedef unsigned long long u64;
typedef GAS u64 gu64;
#define RLX_AGENT __ATOMIC_RELAXED, __HIP_MEMORY_SCOPE_AGENT
#define DEVI __device__ __forceinline__

constexpr unsigned WQ_HEAD = 81920;
constexpr unsigned WO_CG = 102400;
constexpr unsigned XIMG = 2621440;
constexpr unsigned XT = 45056, XIM2 = 64 * XT;
constexpr unsigned VOCT = 352;
constexpr unsigned TILE = 2944, T_K2 = 1024, T_V = 1536;
constexpr unsigned UNIT = 4 * TILE;
constexpr unsigned KV_LAYER = 32 * 64 * TILE;
constexpr unsigned OFF_FLAG = 0;
constexpr unsigned OFF_F1 = 4096, OFF_F2 = OFF_F1 + 32768;
constexpr unsigned OFF_KF = OFF_F2 + 32768;
constexpr unsigned OFF_WQ = OFF_KF + 4 * 32 * 16 * 128;
constexpr unsigned OFF_WO = OFF_WQ + 2 * 32 * WQ_HEAD;
constexpr unsigned OFF_X = OFF_WO + 2 * 8 * WO_CG;
constexpr unsigned OFF_O = OFF_X + 4 * XIM2;
constexpr unsigned OFF_KV = OFF_O + 4 * XIMG;
constexpr unsigned WS_USED = OFF_KV + 4 * KV_LAYER;
constexpr unsigned OFF_DBG = WS_USED;
constexpr unsigned SENT = 0xFFFFFFFFu;
constexpr unsigned OOB = 0xE0000000u;

constexpr unsigned L_A = 0;
constexpr unsigned L_U = 102400;
constexpr unsigned L_M = L_U + 5 * UNIT;
constexpr unsigned L_C = L_M + 512;
constexpr unsigned L_T = L_C + 1024;
constexpr unsigned LDS_BYTES = 163840;
enum { C_RDY = 0, C_DONE = 16, C_WO = 32, C_OP = 33, C_WQ = 34, C_RED = 38, C_GO = 46, C_PR = 47, C_W0 = 48, C_X0 = 49, C_INIT = 50, C_SEEN = 51, C_CS = 52, C_PUB = 53, C_F1 = 59, C_OL = 55, C_OS = 56, C_XG = 57, C_QX = 60, C_MG = 42 };

constexpr unsigned kSpinCap = 1u << 15;
#define NODOT 0
#define ABL 0
#define XPG 2
#define OWD 16
#define HPRIO 1
#define WIN 10
#define TOUCH 0
#define YPRI 2
#define ONAP 2
#define HNAP 2
#define GPRI 1
#define OLN 4
#define PREF 1
#define RWIN 3
#define XI0 8
#define XD 12
#define OSN 8
#define ODRAIN 0
#define OSLP 6
#define XSLP 4
#define MIX 1
#define LAG 0
#define XEARLY 1
#define STREAM 0
#define RPOS 1
#define RSLP 2
#define X0SLP 10
#define LOE 0
#define OLI 0
#define LNAP 8
#define OGAP 0
#define XGAP 0
#define OPP 2
#define YMASK 1
#define APRI 0
#define BPRI 0
#define PMASK 0x6DB6
#define YDLY 0
#define PGATE 4
#define PPRI 1
#define FPOS 1
#define TFETCH 0
#define WOE 40
#define GOAT 36
#define XDRAIN 0
#define PACE 3
#define XPD 16
#define MAPC 2
#define SUM_LIMIT 1.0e18f

DEVI unsigned pack2(float a, float b) {
  bf16x2 v = {(__bf16)a, (__bf16)b};
  return __builtin_bit_cast(unsigned, v);
}
DEVI float sq2(unsigned u, float acc) {
  const bf16x2 p = __builtin_bit_cast(bf16x2, u);
  return __builtin_amdgcn_fdot2_f32_bf16(p, p, acc, false);
}
DEVI float bf_lo(unsigned u) { return __uint_as_float(u << 16); }
DEVI float bf_hi(unsigned u) { return __uint_as_float(u & 0xffff0000u); }
DEVI f32x16 mfma32(u32x4 a, u32x4 b, f32x16 c) {
  return __builtin_amdgcn_mfma_f32_32x32x16_bf16(__builtin_bit_cast(bf16x8, a), __builtin_bit_cast(bf16x8, b), c, 0, 0, 0);
}
DEVI f32x16 mfma32k8(u32x2 a, u32x2 b, f32x16 c) {
  return __builtin_amdgcn_mfma_f32_32x32x8bf16_1k(__builtin_bit_cast(s16x4, a), __builtin_bit_cast(s16x4, b), c, 0, 0, 0);
}
DEVI f32x4 mfma16(u32x4 a, u32x4 b, f32x4 c) {
  return __builtin_amdgcn_mfma_f32_16x16x32_bf16(__builtin_bit_cast(bf16x8, a), __builtin_bit_cast(bf16x8, b), c, 0, 0, 0);
}
DEVI f32x16 pv_lo(u32x4 v, const f32x16& p, f32x16 o) {
  f32x8 h = __builtin_shufflevector(p, p, 0, 1, 2, 3, 4, 5, 6, 7);
  return __builtin_amdgcn_smfmac_f32_32x32x32_bf16(__builtin_bit_cast(bf16x8, v), __builtin_bit_cast(bf16x16, h), o, 0xDDDD, 0, 0);
}
DEVI f32x16 pv_hi(u32x4 v, const f32x16& p, f32x16 o) {
  f32x8 h = __builtin_shufflevector(p, p, 8, 9, 10, 11, 12, 13, 14, 15);
  return __builtin_amdgcn_smfmac_f32_32x32x32_bf16(__builtin_bit_cast(bf16x8, v), __builtin_bit_cast(bf16x16, h), o, 0xDDDD, 0, 0);
}
DEVI u32x4 lds_r128(unsigned a) { return *(LAS u32x4*)a; }
DEVI u32x2 lds_r64(unsigned a) { return *(LAS u32x2*)a; }
DEVI void lds_w128(unsigned a, u32x4 v) { *(LAS u32x4*)a = v; }
DEVI void lds_w64(unsigned a, u32x2 v) { *(LAS u32x2*)a = v; }
DEVI void lds_w16(unsigned a, float v) { *(LAS __bf16*)a = (__bf16)v; }
DEVI float half_lo(float v) { return __uint_as_float(__builtin_amdgcn_permlane32_swap(__float_as_uint(v), __float_as_uint(v), false, false)[0]); }
DEVI float half_hi(float v) { return __uint_as_float(__builtin_amdgcn_permlane32_swap(__float_as_uint(v), __float_as_uint(v), false, false)[1]); }
DEVI unsigned umax(unsigned a, unsigned b) { return a > b ? a : b; }
DEVI unsigned umax4(unsigned m, u32x4 v) { return umax(umax(umax(umax(m, v.x), v.y), v.z), v.w); }
DEVI bool unwritten(unsigned m) { return __builtin_amdgcn_ballot_w64(m == SENT) != 0; }
DEVI unsigned clean(unsigned v) { return v < SENT ? v : SENT - 1; }
DEVI u32x4 clean4(u32x4 v) { return (u32x4){clean(v.x), clean(v.y), clean(v.z), clean(v.w)}; }
DEVI u32x2 clean2(u32x2 v) { return (u32x2){clean(v.x), clean(v.y)}; }
DEVI unsigned vpos(int d) {
  const unsigned q = ((14u | 0u << 5 | 4u << 10 | 18u << 15 | 8u << 20) >> ((d >> 2) * 5 & 31)) & 31u;
  return d < 20 ? q + (d & 3) : (d == 20 ? 13u : 12u);
}
DEVI void lds_w16lo(unsigned a, unsigned v) { *(LAS u16*)a = (u16)v; }
DEVI void lds_w16hi(unsigned a, unsigned v) { *(LAS u16*)a = (u16)(v >> 16); }
DEVI float sum2(unsigned u, float acc) {
  const bf16x2 ones = {(__bf16)1.0f, (__bf16)1.0f};
  return __builtin_amdgcn_fdot2_f32_bf16(__builtin_bit_cast(bf16x2, u), ones, acc, false);
}
DEVI void split3(float v, unsigned& hi, unsigned& mid, unsigned& lo) {
  hi = __float_as_uint(v) & 0xffff0000u;
  const float r1 = v - __uint_as_float(hi);
  mid = __float_as_uint(r1) & 0xffff0000u;
  lo = __float_as_uint(r1 - __uint_as_float(mid)) & 0xffff0000u;
}
#define SB() __builtin_amdgcn_sched_barrier(0)
DEVI void bar_lds() {
  asm volatile("s_waitcnt lgkmcnt(0)\n\ts_barrier" ::: "memory");
}
DEVI void drain_vm() { asm volatile("s_waitcnt vmcnt(0)" ::: "memory"); }
#define DMA_ASM(d, v, r, o) "s_mov_b32 m0, " d "\n\ts_nop 3\n\tbuffer_load_dwordx4 " v ", " r ", " o " offen sc1 lds\n\t"
DEVI void dma1k(unsigned lds_dst, unsigned voff, u32x4 srd, unsigned soff) {
  unsigned keep;
  asm volatile("s_mov_b32 %0, m0\n\t" DMA_ASM("%1", "%2", "%3", "%4") "s_mov_b32 m0, %0"
               : "=&s"(keep) : "s"(__builtin_amdgcn_readfirstlane(lds_dst)), "v"(voff), "s"(srd), "s"(__builtin_amdgcn_readfirstlane(soff)) : "memory");
}
constexpr unsigned NO_LDS = 0x30000;
typedef __amdgpu_buffer_rsrc_t rsrc_t;
DEVI u32x4 ld128(rsrc_t r, unsigned voff, unsigned soff) { return __builtin_amdgcn_raw_buffer_load_b128(r, voff, soff, 16); }
DEVI u32x2 ld64(rsrc_t r, unsigned voff, unsigned soff) { return __builtin_amdgcn_raw_buffer_load_b64(r, voff, soff, 16); }
template <int AUX> DEVI void st128a(u32x4 v, rsrc_t r, unsigned voff, unsigned soff) {
  __builtin_amdgcn_raw_buffer_store_b128(v, r, voff, soff, AUX);
  asm volatile("s_nop 1" :: "v"(v) : "memory");
}
DEVI void st128(u32x4 v, rsrc_t r, unsigned voff, unsigned soff) { st128a<16>(v, r, voff, soff); }
DEVI void st64(u32x2 v, rsrc_t r, unsigned voff, unsigned soff) { __builtin_amdgcn_raw_buffer_store_b64(v, r, voff, soff, 16); }

DEVI bool give_up(unsigned& spins) {
  asm volatile("s_sleep 2" ::: "memory");
  return ++spins > kSpinCap;
}
DEVI u32x2 next_tag(u32x2 g) {
  u64 v = ((u64)g.y << 32 | g.x) + 1;
  v += v == ~0ull;
  return (u32x2){(unsigned)v, (unsigned)(v >> 32)};
}
DEVI void wait_tag(rsrc_t rs, unsigned off, u32x2 tag) {
  for (unsigned spins = 0;;) {
    const u32x2 v = ld64(rs, off, 0);
    if (__builtin_amdgcn_ballot_w64(off != OOB && (v.x != tag.x || v.y != tag.y)) == 0 || give_up(spins)) break;
  }
  asm volatile("" ::: "memory");
}

#define STAMP(i) do {} while (0)
#define STAMP_OUT() do {} while (0)

DEVI void transpose8x4(const u32x2 (&r)[8], u32x4 (&o)[4]) {
#pragma unroll
  for (int p = 0; p < 4; ++p) {
    o[0][p] = __builtin_amdgcn_perm(r[2 * p + 1].x, r[2 * p].x, 0x05040100u);
    o[1][p] = __builtin_amdgcn_perm(r[2 * p + 1].x, r[2 * p].x, 0x07060302u);
    o[2][p] = __builtin_amdgcn_perm(r[2 * p + 1].y, r[2 * p].y, 0x05040100u);
    o[3][p] = __builtin_amdgcn_perm(r[2 * p + 1].y, r[2 * p].y, 0x07060302u);
  }
}

DEVI void wq_item_load(u32x2 (&r)[8], rsrc_t rw, int h, int kh, int qd, bool on) {
  const int mt = qd >> 3, rq = qd & 7;
  const int col = h * 20 + (rq < 5 ? mt * 640 + 4 * rq : 1280 + mt * 12 + 4 * (rq - 5));
  const unsigned off = (on && qd < 15) ? kh * 8 * 3840 + col * 2 : OOB;
#pragma unroll
  for (int j = 0; j < 8; ++j) r[j] = __builtin_amdgcn_raw_buffer_load_b64(rw, off, j * 3840, 0);
}
DEVI unsigned wq_item_pos(int kh, int qd) { return (kh >> 1) * 2048 + (qd >> 3) * 1024 + (kh & 1) * 512 + (qd & 7) * 64; }

struct Frag { u32x4 k1; u32x2 k2; u32x4 va, vb; };

template <bool QK, bool LDK, bool LDV>
DEVI void att_step(f32x16& cur, f32x16& oth, f32x16& o, Frag& f, const u32x4& q1, const u32x2& q2, const f32x16& cin,
                   unsigned a_vb, unsigned a_k1, unsigned a_k2, unsigned a_va) {
  o = pv_hi(f.vb, oth, o);
  f.vb = lds_r128(a_vb);
  SB();
#pragma unroll
  for (int i = 0; i < 4; ++i) cur[i] = __builtin_amdgcn_exp2f(cur[i]);
  SB();
  if (QK) oth = mfma32(f.k1, q1, cin);
  if (LDK) f.k1 = lds_r128(a_k1);
  SB();
#pragma unroll
  for (int i = 4; i < 8; ++i) cur[i] = __builtin_amdgcn_exp2f(cur[i]);
  SB();
  if (QK) oth = mfma32k8(f.k2, q2, oth);
  if (LDK) f.k2 = lds_r64(a_k2);
  SB();
#pragma unroll
  for (int i = 8; i < 12; ++i) cur[i] = __builtin_amdgcn_exp2f(cur[i]);
  SB();
  o = pv_lo(f.va, cur, o);
  if (LDV) f.va = lds_r128(a_va);
  SB();
#pragma unroll
  for (int i = 12; i < 16; ++i) cur[i] = __builtin_amdgcn_exp2f(cur[i]);
  SB();
}
struct Frag2 { u32x4 k1; u32x2 k2; u32x4 va, vb, vo; };
#define EXP4(s, b) do { SB(); _Pragma("unroll") for (int i_ = b; i_ < b + 4; ++i_) s[i_] = __builtin_amdgcn_exp2f(s[i_]); SB(); } while (0)
DEVI void att_step2(f32x16& sa, f32x16& sb, f32x16& oa, f32x16& ob, Frag2& f, const u32x4& qa1, const u32x2& qa2,
                    const u32x4& qb1, const u32x2& qb2, unsigned a_k1, unsigned a_k2, unsigned a_v) {
  const f32x16 z = {0.f, 0.f, 0.f, 0.f, 0.f, 0.f, 0.f, 0.f, 0.f, 0.f, 0.f, 0.f, 0.f, 0.f, 0.f, 0.f};
  ob = pv_hi(f.vo, sb, ob);
  EXP4(sa, 0);
  sb = mfma32(f.k1, qb1, z);
  f.k1 = lds_r128(a_k1);
  EXP4(sa, 4);
  sb = mfma32k8(f.k2, qb2, sb);
  f.k2 = lds_r64(a_k2);
  EXP4(sa, 8);
  oa = pv_lo(f.va, sa, oa);
  EXP4(sa, 12);
  oa = pv_hi(f.vb, sa, oa);
  f.vo = f.vb;
  EXP4(sb, 0);
  sa = mfma32(f.k1, qa1, z);
  f.vb = lds_r128(a_v + 2 * VOCT);
  EXP4(sb, 4);
  sa = mfma32k8(f.k2, qa2, sa);
  EXP4(sb, 8);
  ob = pv_lo(f.va, sb, ob);
  f.va = lds_r128(a_v);
  EXP4(sb, 12);
}
DEVI void bar_if_eq(int a, int b) {
  asm volatile("s_cmp_lg_u32 %0, %1\n\ts_cbranch_scc1 .Lskip%=\n\ts_waitcnt lgkmcnt(0)\n\ts_barrier\n.Lskip%=:" :: "s"(a), "s"(b) : "memory", "scc");
}
DEVI void bar_if_ne(int a, int b) {
  asm volatile("s_cmp_eq_u32 %0, %1\n\ts_cbranch_scc1 .Lskip%=\n\ts_waitcnt lgkmcnt(0)\n\ts_barrier\n.Lskip%=:" :: "s"(a), "s"(b) : "memory", "scc");
}
DEVI void prio_if(int on) {
  asm volatile("s_cmp_eq_u32 %0, 0\n\ts_cbranch_scc1 .Lskip%=\n\ts_setprio 1\n.Lskip%=:" :: "s"(on) : "scc");
}
DEVI void prio0_if(int on) {
  asm volatile("s_cmp_eq_u32 %0, 0\n\ts_cbranch_scc1 .Lskip%=\n\ts_setprio 0\n.Lskip%=:" :: "s"(on) : "scc");
}
DEVI void prio_pat(int y, int n) {
  asm volatile("s_setprio 0\n\ts_bitcmp1_b32 %0, %1\n\ts_cbranch_scc0 .Lskip%=\n\ts_setprio 1\n.Lskip%=:" :: "s"(y ? PMASK : 0), "s"(n) : "scc");
}
DEVI int opaque(int v) { asm volatile("" : "+v"(v)); return v; }

DEVI float max16(const f32x16& s) {
  float m = fmaxf(fmaxf(s[0], s[1]), s[2]);
#pragma unroll
  for (int i = 3; i < 15; i += 2) m = fmaxf(fmaxf(m, s[i]), s[i + 1]);
  return fmaxf(m, s[15]);
}
DEVI void set_shift(u32x2& q2, float m, int hh) {
  unsigned hi, mid, lo;
  split3(-m, hi, mid, lo);
  q2.x = hh ? (hi >> 16) | mid : q2.x;
  q2.y = hh ? (lo >> 16) | 0xFF7F0000u : q2.y;
}
template <bool DIAG> DEVI f32x16 tri_mask() {
  f32x16 c;
#pragma unroll
  for (int i = 0; i < 16; ++i) {
    const int key = (i & 3) + 8 * (i >> 2);
    const u64 above = ((1ull << key) - 1) | ((1ull << (key + 4)) - 1) << 32;
    c[i] = __builtin_amdgcn_inverse_ballot_w64(DIAG ? above : ~above) ? -3.0e38f : 0.f;
  }
  return c;
}

DEVI void ev_wait_if_eq(int a, int b, unsigned addr, unsigned target) {
  unsigned sv, cnt, v;
  asm volatile("s_cmp_lg_u32 %3, %4\n\ts_cbranch_scc1 .Lgo%=\n\ts_mov_b32 %1, 0x100000\n\ts_nop 7\n\ts_nop 3\n"
               ".Lspin%=:\n\tds_read_b32 %2, %5\n\ts_waitcnt lgkmcnt(0)\n\tv_readfirstlane_b32 %0, %2\n\t"
               "s_cmp_ge_u32 %0, %6\n\ts_cbranch_scc1 .Lgo%=\n\ts_sleep 1\n\ts_sub_u32 %1, %1, 1\n\ts_cbranch_scc0 .Lspin%=\n"
               ".Lgo%=:"
               : "=&s"(sv), "=&s"(cnt), "=&v"(v) : "s"(a), "s"(b), "v"(addr), "s"(target) : "memory", "scc");
}
DEVI void unit_gate(int n, unsigned evv) {
  unsigned sv, cnt, v, a;
  asm volatile("s_bitcmp1_b32 0x3200, %4\n\ts_cbranch_scc0 .Lgo%=\n\ts_nop 7\n\ts_nop 3\n\t"
               "v_lshl_add_u32 %3, %4, 2, %5\n\tv_mov_b32 %2, 1\n\ts_mov_b64 exec, 1\n\tds_add_u32 %3, %2 offset:%6\n\ts_mov_b64 exec, -1\n\t"
               "s_mov_b32 %1, 0x100000\n"
               ".Lspin%=:\n\tds_read_b32 %2, %3 offset:%7\n\ts_waitcnt lgkmcnt(0)\n\tv_readfirstlane_b32 %0, %2\n\t"
               "s_cmp_ge_u32 %0, 4\n\ts_cbranch_scc1 .Lgo%=\n\ts_sleep 1\n\ts_sub_u32 %1, %1, 1\n\ts_cbranch_scc0 .Lspin%=\n"
               ".Lgo%=:"
               : "=&s"(sv), "=&s"(cnt), "=&v"(v), "=&v"(a) : "s"(n), "v"(evv), "i"(C_DONE * 4), "i"(C_RDY * 4 + 4) : "memory", "scc");
}
DEVI void ev_post(unsigned addr, unsigned n = 1) {
  asm volatile("s_mov_b64 exec, 1\n\tds_add_u32 %0, %1\n\ts_mov_b64 exec, -1" :: "v"(addr), "v"(n) : "memory");
}
DEVI unsigned ev_peek(unsigned addr) { return *(volatile LAS unsigned*)addr; }
template <int NAP = 1> DEVI void ev_wait_from(unsigned seen, unsigned addr, unsigned target) {
  unsigned sv, cnt;
  asm volatile("s_nop 0\n\tv_readfirstlane_b32 %0, %2\n\ts_cmp_ge_u32 %0, %4\n\ts_cbranch_scc1 .Lgo%=\n\t"
               "s_mov_b32 %1, 0x100000\n"
               ".Lspin%=:\n\ts_sleep %5\n\tds_read_b32 %2, %3\n\ts_sub_u32 %1, %1, 1\n\ts_cbranch_scc1 .Lout%=\n\t"
               "s_waitcnt lgkmcnt(0)\n\tv_readfirstlane_b32 %0, %2\n\t"
               "s_cmp_ge_u32 %0, %4\n\ts_cbranch_scc0 .Lspin%=\n"
               ".Lout%=:\n\ts_waitcnt lgkmcnt(0)\n"
               ".Lgo%=:"
               : "=&s"(sv), "=&s"(cnt), "+&v"(seen)
               : "v"(addr), "s"(__builtin_amdgcn_readfirstlane(target)), "i"(NAP) : "memory", "scc");
}
template <int NAP = 1> DEVI void ev_wait(unsigned addr, unsigned target) { ev_wait_from<NAP>(ev_peek(addr), addr, target); }

DEVI void kv_publish(unsigned l0, unsigned l5, rsrc_t rs, unsigned un, unsigned slot) {
  u32x4 g[6];
#pragma unroll
  for (int i = 0; i < 6; ++i) g[i] = lds_r128(L_U + slot * UNIT + l0 + i * 2048);
#pragma unroll
  for (int i = 0; i < 6; ++i) st128(g[i], rs, i < 5 ? l0 : l5, OFF_KV + un * UNIT + i * 2048);
}
DEVI unsigned unit_home(int n) { return L_U + (n < 5 ? n : n - 13) * UNIT; }
DEVI void kv_fetch(unsigned l16, u32x4 srd, unsigned un, unsigned dst, int first, int step) {
  for (int i = first; i < 12; i += step) {
    const unsigned o = i < 11 ? i * 1024 : UNIT - 1024;
    dma1k(dst + o, l16, srd, OFF_KV + un * UNIT + o);
  }
}
DEVI int kv_order(int c, int j) { return c < 4 ? (j < 4 ? (c - j) & 3 : 4 + ((c - j) & 3)) : (j < 5 ? (c - j) & 3 : 4 + ((c - j) & 3)); }
DEVI void kv_fake(unsigned l0, unsigned l5, unsigned slot) {
#pragma unroll
  for (int i = 0; i < 6; ++i) {
    const unsigned o = (l0 + i * 2048) % TILE, one = (o >= 1280 && o < T_V) ? 0x3F800000u : 0u;
    if (i < 5 || l5 != OOB) lds_w128(L_U + slot * UNIT + l0 + i * 2048, (u32x4){0u, one, 0u, one});
  }
}
DEVI unsigned o_piece(unsigned q, int l, int c, int h) { return OFF_O + l * XIMG + c * 327680 + h * 10240 + q * 1024; }
DEVI unsigned x_piece(unsigned q, int l, int c, int h) { return OFF_X + l * XIM2 + c * 8 * XT + h * (XT / 4) + q * 1024; }
DEVI void add_colsum(const u32x4 (&wn)[20], int u, int lane, unsigned dst) {
  float s0 = 0.f, s1 = 0.f;
  const bf16x2 ones = {(__bf16)1.0f, (__bf16)1.0f};
#pragma unroll
  for (int i = 0; i < 20; ++i) {
    const unsigned a = wn[i].x, b = wn[i].y, c = wn[i].z, d = wn[i].w;
    s0 = __builtin_amdgcn_fdot2_f32_bf16(__builtin_bit_cast(bf16x2, a), ones, s0, false);
    s1 = __builtin_amdgcn_fdot2_f32_bf16(__builtin_bit_cast(bf16x2, b), ones, s1, false);
    s0 = __builtin_amdgcn_fdot2_f32_bf16(__builtin_bit_cast(bf16x2, c), ones, s0, false);
    s1 = __builtin_amdgcn_fdot2_f32_bf16(__builtin_bit_cast(bf16x2, d), ones, s1, false);
  }
  s0 += s1;
  s0 = half_lo(s0) + half_hi(s0);
  if (lane < 32) __hip_atomic_fetch_add((LAS float*)(dst + (u & 1) * 128 + lane * 4), s0, __ATOMIC_RELAXED, __HIP_MEMORY_SCOPE_WORKGROUP);
}

DEVI void weight_share(int task, int lane, int c, int h, const u16* wq, const u16* wo, rsrc_t rs, unsigned bounce, bool bar) {
  const int m2 = task >> 2, wi = task & 3;
  u32x2 r[8];
  u32x4 o[4];
  unsigned dst, pos, np;
  if (wi < 3) {
    const rsrc_t rw = __builtin_amdgcn_make_buffer_rsrc((void*)wq, 0, 640 * 3840, 0x00020000);
    wq_item_load(r, rw, h, c * 10 + wi * 4 + (lane >> 4), lane & 15, wi < 2 || lane < 32);
    pos = wq_item_pos(lane >> 4, lane & 15);
    dst = OFF_WQ + (m2 * 32 + h) * WQ_HEAD + (c * 10 + wi * 4) * 1024;
    np = wi < 2 ? 4 : 2;
  } else {
    const int b32 = (h >> 3) * 8 + c, p = b32 * 4 + (lane >> 4), nt = p % 5, q = (p / 5) * 4 + ((lane >> 2) & 3), g = lane & 3;
    const int c4 = nt < 4 ? (nt >> 1) * 8 + g * 2 + (nt & 1) : 16 + g;
    const rsrc_t rw = __builtin_amdgcn_make_buffer_rsrc((void*)wo, 0, 640 * 1280, 0x00020000);
    const int k0 = q < 64 ? (q >> 1) * 20 + (q & 1) * 4 : (q - 64) * 40 + 16, dk = q < 64 ? 8 : 20;
    const unsigned wof = b32 < 25 ? k0 * 1280 + ((h & 7) * 80 + c4 * 4) * 2 : OOB;
#pragma unroll
    for (int j = 0; j < 4; ++j) {
      r[j] = __builtin_amdgcn_raw_buffer_load_b64(rw, wof, j * 1280, 0);
      r[4 + j] = __builtin_amdgcn_raw_buffer_load_b64(rw, b32 < 25 ? wof + dk * 1280 : OOB, j * 1280, 0);
    }
    pos = lane * 64;
    dst = OFF_WO + (m2 * 8 + (h & 7)) * WO_CG + b32 * 4096;
    np = b32 < 25 ? 4 : 0;
  }
  if (bar) bar_lds();
  transpose8x4(r, o);
#pragma unroll
  for (int i = 0; i < 4; ++i) if (pos < np * 1024) lds_w128(bounce + pos + i * 16, o[i]);
#pragma unroll
  for (int i = 0; i < 4; ++i) o[i] = lds_r128(bounce + i * 1024 + lane * 16);
#pragma unroll
  for (int i = 0; i < 4; ++i) st128(o[i], rs, (unsigned)i < np ? lane * 16 : OOB, dst + i * 1024);
}

__global__ void __launch_bounds__(768, 3) main_kernel(const u16* __restrict__ x, const u16* __restrict__ wqA, const u16* __restrict__ woA,
                                                      const u16* __restrict__ wqB, const u16* __restrict__ woB,
                                                      char* __restrict__ ws, u16* __restrict__ out) {
  const int tid0 = threadIdx.x;
  const int w = __builtin_amdgcn_readfirstlane(tid0 >> 6);
  const int y = (w >> 2) & 1;
  const int tt = y ? 7 - w : w + 4;
  const int xg = blockIdx.x & 7, xi = blockIdx.x >> 3;
  const int c0 = ((xg << MAPC) & 7) | (xi & ((1 << MAPC) - 1));
  const int c = MIX ? ((c0 & 1) << 2) | (c0 >> 1) : c0;
  const int h = ((xg >> (3 - MAPC)) << (5 - MAPC)) | (xi >> MAPC);
#define LANE_VARS const int tid = opaque(tid0), lane = tid & 63, t = lane & 31, hh = lane >> 5; (void)t; (void)hh
  const rsrc_t rs = __builtin_amdgcn_make_buffer_rsrc(ws, 0, WS_USED, 0x00020000);
  const rsrc_t ro = __builtin_amdgcn_make_buffer_rsrc(out, 0, XIMG, 0x00020000);
  const u32x4 srd = {(unsigned)(size_t)ws, (unsigned)((size_t)ws >> 32) & 0xffffu, WS_USED, 0x00020000u};
  const f32x16 zero16 = {0.f, 0.f, 0.f, 0.f, 0.f, 0.f, 0.f, 0.f, 0.f, 0.f, 0.f, 0.f, 0.f, 0.f, 0.f, 0.f};
  STAMP(0);
  if (tid0 < 384) *(LAS unsigned*)(L_M + tid0 * 4) = 0u;

  if (w >= 8) {
    const int u = w - 8, lane = tid0 & 63, me = h * 8 + c;
    const unsigned l16 = lane * 16, l4 = u * 1024 + l16;
    const unsigned l0 = (u & 1) * 1024 + l16, l5 = l0 + 10240 < UNIT ? l0 : OOB;
    const u32x4 sv = {SENT, SENT, SENT, SENT};
    __builtin_amdgcn_s_setprio(HPRIO);
    u32x2 tag = ld64(rs, 0, OFF_FLAG);
    if (u == 0) {
      const rsrc_t rx = __builtin_amdgcn_make_buffer_rsrc((void*)x, 0, XIMG, 0x00020000);
      const int row = c * 256 + h * 8 + (lane & 7), p = lane >> 3;
      u32x4 v[10];
#pragma unroll
      for (int i = 0; i < 10; ++i) v[i] = __builtin_amdgcn_raw_buffer_load_b128(rx, row * 1280 + p * 16, i * 128, 0);
      st128(sv, rs, lane < 16 ? OFF_F1 + p * 32768 + me * 128 + (lane & 7) * 16 : OOB, 0);
      st128(sv, rs, OFF_KF + (((p >> 1) * 32 + h) * 16 + 2 * c + (p & 1)) * 128 + (lane & 7) * 16, 0);
      bar_lds();
      const unsigned xd = OFF_X + (row >> 5) * XT;
      float ss = 0.f, sm = 0.f;
#pragma unroll
      for (int i = 0; i < 10; ++i) {
        st128(clean4(v[i]), rs, xd + p * 512 + (row & 31) * 16, i * 4096);
        ss = sq2(v[i].x, ss); ss = sq2(v[i].y, ss); ss = sq2(v[i].z, ss); ss = sq2(v[i].w, ss);
        sm = sum2(v[i].x, sm); sm = sum2(v[i].y, sm); sm = sum2(v[i].z, sm); sm = sum2(v[i].w, sm);
      }
      st128(clean4((u32x4){__float_as_uint(sm), __float_as_uint(ss), 0u, 0u}), rs, xd + 40960 + (row & 31) * 16 + p * 512, 0);
      tag = next_tag(tag);
      lds_w64(L_T, tag);
      drain_vm();
      ev_wait(L_C + C_W0 * 4, 3);
      st64(tag, rs, lane == 0 ? 0u : OOB, OFF_F1 + me * 128);
      ev_post(L_C + C_F1 * 4);
      STAMP(41);
      wait_tag(rs, lane < 40 ? OFF_F1 + (lane < 32 ? lane * 8 + c : h * 8 + lane - 32) * 128 : OOB, tag);
      ev_post(L_C + C_X0 * 4);
      STAMP(42);
    } else {
      if (RPOS == 0) for (unsigned q = 0; q < 10u + (u == 3); ++q) st128(sv, rs, l16, u < 3 ? o_piece(q, u - 1, c, h) : x_piece(q, 1, c, h));
      bar_lds();
      tag = next_tag(tag);
      ev_wait(L_C + C_X0 * 4, 1);
    }
    {
      u32x4 wn[20];
#pragma unroll
      for (int i = 0; i < 20; ++i) wn[i] = ld128(rs, l4, OFF_WQ + h * WQ_HEAD + i * 4096);
#pragma unroll
      for (int q = 0; q < 4; ++q) {
#pragma unroll
        for (int i = 5 * q; i < 5 * q + 5; ++i) lds_w128(L_A + l4 + i * 4096, wn[i]);
        ev_post(L_C + (C_WQ + q) * 4);
      }
      STAMP(45);
      if (RPOS == 1 && u) for (unsigned q = 0; q < 10u + (u == 3); ++q) { st128(sv, rs, l16, u < 3 ? o_piece(q, u - 1, c, h) : x_piece(q, 1, c, h)); __builtin_amdgcn_s_sleep(RSLP); }
      add_colsum(wn, u, lane, L_M);
      ev_post(L_C + C_CS * 4);
      drain_vm();
      ev_post(L_C + C_INIT * 4);
      if (u == 3) {
        ev_wait<4>(L_C + C_INIT * 4, 12);
        st64(tag, rs, lane == 0 ? 0u : OOB, OFF_F2 + me * 128);
        STAMP(43);
      }
    }
    for (int layer = 0; layer < 4; ++layer) {
      const int m = layer & 1;
      const unsigned ev = L_C + layer * 256;
      const unsigned ub = (layer * 32 + h) * 16, wo_src = OFF_WO + (m * 8 + (h & 7)) * WO_CG;
      const unsigned hi = u < 2;
      const unsigned own = m ? 1 - hi : 3 + hi;
      const unsigned wq_next = layer < 3 ? OFF_WQ + ((m ^ 1) * 32 + h) * WQ_HEAD : OOB;
      if (LNAP) ev_wait<LNAP>(ev + C_PR * 4, 1);
      if ((TFETCH || !hi) && layer == 2) {
        ev_wait<2>(ev + C_PR * 4, 8);
        for (int p = TFETCH ? u : u - 2; p < WOE; p += TFETCH ? 4 : 2) dma1k(L_A + p * 1024, l16, srd, wo_src + p * 1024);
      }
      if (TFETCH) {
        for (int k = 0; k < 2; ++k) {
          const unsigned un = ub + 2 * (m ? kv_order(c, 1) : (c - 1) & 7) + 1 - k, slot = m ? 2 + k : 2 - k;
          ev_wait(ev + (C_PUB + k) * 4, 4);
          if (k == 0) { STAMP(2 + layer * 10); ev_wait(ev + C_PR * 4, 8); }
          else if (m) ev_wait(ev + C_DONE * 4, 8);
          if (m || c > 0) {
            wait_tag(rs, lane == 0 ? OFF_KF + un * 128 + u * 8 : OOB, tag);
            for (int i = 0; i < 3; ++i) {
              const unsigned o = u * TILE + (i < 2 ? i * 1024 : TILE - 1024);
              dma1k(L_U + slot * UNIT + o, l16, srd, OFF_KV + un * UNIT + o);
            }
            drain_vm();
          } else if (hi) {
            kv_fake(l0, l5, slot);
          }
          ev_post(ev + (C_RDY + slot) * 4);
          if (k == 0) STAMP(1 + layer * 10);
        }
      } else {
      ev_wait(ev + (C_PUB + 1 - hi) * 4, PGATE);
      STAMP(2 + layer * 10);
      {
        const unsigned un = ub + 2 * (m ? kv_order(c, 1) : (c - 1) & 7) + hi, slot = m ? 3 - hi : 1 + hi;
        ev_wait(ev + C_PR * 4, 8);
        if (m || c > 0) {
          wait_tag(rs, lane < 4 ? OFF_KF + un * 128 + lane * 8 : OOB, tag);
          if (m && !hi) ev_wait(ev + C_DONE * 4, 8);
          kv_fetch(l16, srd, un, L_U + slot * UNIT, u & 1, 2);
          drain_vm();
        } else {
          kv_fake(l0, l5, slot);
        }
        ev_post(ev + (C_RDY + slot) * 4, 2);
      }
      }
      STAMP(3 + layer * 10);
      if (m == 0) {
        if (layer == 0) {
          wait_tag(rs, OFF_F2 + (lane < 32 ? lane * 8 + c : (((lane >> 3) & 3) * 8 + (h & 7)) * 8 + (lane & 7)) * 128, tag);
          ev_post(L_C + C_SEEN * 4);
          STAMP(44);
          ev_wait(ev + C_PUB * 4, 4); ev_wait(ev + (C_PUB + 1) * 4, 4);
          for (int p = u; p < 100; p += 4) dma1k(L_A + p * 1024, l16, srd, wo_src + p * 1024);
        } else if (TFETCH) {
          for (int p = WOE + u; p < 100; p += 4) dma1k(L_A + p * 1024, l16, srd, wo_src + p * 1024);
        } else if (hi) {
          ev_wait(ev + (C_RDY + 3) * 4, 4);
          for (int p = WOE + u; p < 100; p += 2) dma1k(L_A + p * 1024, l16, srd, wo_src + p * 1024);
        }
      } else {
        ev_wait(ev + C_PUB * 4, 4); ev_wait(ev + (C_PUB + 1) * 4, 4);
        ev_post(ev + (C_RDY + 10) * 4); ev_post(ev + (C_RDY + 14) * 4);
        for (int n = 4 + u; n < 17; n += 4) {
          if (n >= 13 && layer == 1) {
            for (unsigned q = 0; q < 10u + (u >> 1); ++q) {
              st128(sv, rs, l16, u < 2 ? o_piece(q, 2 + u, c, h) : x_piece(q, u, c, h));
              __builtin_amdgcn_s_sleep(2);
            }
          }
          if (n < 16) {
            const unsigned un2 = ub + 2 * kv_order(c, n >> 1) + 1 - (n & 1);
            wait_tag(rs, lane < 4 ? OFF_KF + un2 * 128 + lane * 8 : OOB, tag);
            if (n == 4) ev_wait(ev + C_DONE * 4, 8);
            if (n >= 13) ev_wait<8>(ev + (C_DONE + 3) * 4, 8);
            kv_fetch(l16, srd, un2, unit_home(n), 0, 1);
          }
          drain_vm();
          if (n != 8) ev_post(ev + (C_RDY + (n < 8 ? n : n < 13 ? 8 : 13)) * 4, n < 8 ? 4 : 1);
        }
        if (layer == 3) {
          ev_wait<4>(ev + (C_RDY + 13) * 4, 4);
          st64(tag, rs, (tid0 == 512 && blockIdx.x == 0) ? 0u : OOB, OFF_FLAG);
        }
      }
      if (m == 0) {
        drain_vm();
        ev_post(ev + C_WO * 4);
        STAMP(6 + layer * 10);
        ev_wait<4>(ev + C_OS * 4, 8);
        ev_wait<2>(ev + C_OL * 4, OLN);
      }
      u32x4 wn[20];
#pragma unroll
      for (int i = 0; i < 20; ++i) wn[i] = ld128(rs, l4, wq_next + i * 4096);
      SB();
      if (m) {
        ev_wait<16>(ev + (C_DONE + 9) * 4, 8);
        for (int p = u; p < 52; p += 4) { dma1k(L_A + p * 1024, l16, srd, wo_src + p * 1024); __builtin_amdgcn_s_sleep(1); }
        ev_wait<8>(ev + (C_DONE + 13) * 4, 8);
        for (int p = 52 + u; p < 100; p += 4) dma1k(L_A + p * 1024, l16, srd, wo_src + p * 1024);
        drain_vm();
        ev_post(ev + C_WO * 4);
        STAMP(6 + layer * 10);
        if (layer == 3) break;
      }
      STAMP(7 + layer * 10);
      if (m) { ev_wait<8>(ev + C_OS * 4, 1); ev_wait<4>(ev + C_OL * 4, 4); }
      ev_wait(ev + C_OP * 4, 8);
      STAMP(8 + layer * 10);
#pragma unroll
      for (int q = 0; q < 4; ++q) {
#pragma unroll
        for (int i = 5 * q; i < 5 * q + 5; ++i) lds_w128(L_A + l4 + i * 4096, wn[i]);
        if (q == 3) { ev_wait(ev + C_XG * 4, STREAM ? 4 : 8); ev_wait(ev + (C_XG + STREAM) * 4, 4); }
        ev_post(ev + 256 + (C_WQ + q) * 4);
      }
      if (layer == 0) { add_colsum(wn, u, lane, L_M + 256); ev_post(ev + 256 + C_CS * 4); }
      STAMP(9 + layer * 10);
    }
    STAMP_OUT();
    return;
  }

  {
    const int lane = tid0 & 63;
    if (w < 3) {
      weight_share(w, lane, c, h, wqA, woA, rs, L_U + w * 4096, true);
      drain_vm();
      ev_post(L_C + C_W0 * 4);
    } else {
      const unsigned id = (blockIdx.x * 5 + w - 3) * 64 + lane;
      const rsrc_t r1 = __builtin_amdgcn_make_buffer_rsrc((void*)wqB, 0, 640 * 3840, 0x00020000);
      const rsrc_t r2 = __builtin_amdgcn_make_buffer_rsrc((void*)woA, 0, 640 * 1280, 0x00020000);
      const rsrc_t r3 = __builtin_amdgcn_make_buffer_rsrc((void*)woB, 0, 640 * 1280, 0x00020000);
      const u32x4 p0 = __builtin_amdgcn_raw_buffer_load_b128(r1, id * 16, 0, 0), p1 = __builtin_amdgcn_raw_buffer_load_b128(r1, id * 16, 1310720, 0);
      const u32x4 p2 = __builtin_amdgcn_raw_buffer_load_b128(r2, id * 16, 0, 0), p3 = __builtin_amdgcn_raw_buffer_load_b128(r3, id * 16, 0, 0);
      bar_lds();
      asm volatile("" :: "v"(p0), "v"(p1), "v"(p2), "v"(p3));
      ev_wait(L_C + C_F1 * 4, 1);
      weight_share(w, lane, c, h, wqB, w == 3 ? woA : woB, rs, L_U + w * 4096, false);
    }
    STAMP(41);
    ev_wait(L_C + C_X0 * 4, 1);
    if (X0SLP) __builtin_amdgcn_s_sleep(X0SLP);
    STAMP(42);
  }

  for (int layer = 0; layer < 4; ++layer) {
    const int m = layer & 1;
    const unsigned ev = L_C + layer * 256;
    u32x4 q1, k1; u32x2 q2, k2;
    {
      LANE_VARS;
      __builtin_amdgcn_s_setprio(GPRI);
      if (YDLY && y) __builtin_amdgcn_s_sleep(YDLY);
      STAMP(1 + layer * 10);
      const unsigned xoff = OFF_X + layer * XIM2 + (c * 8 + tt) * XT;
      const unsigned xl = lane * 16;
#define XLD(k) ld128(rs, xl + ((k) & 3) * 1024, xoff + ((k) >> 2) * 4096)
#define XISS(ks) (XI0 + 2 * (ks) < XD + (ks) ? XI0 + 2 * (ks) : XD + (ks))
      u32x4 xb[40];
      u32x4 sq[4];
#pragma unroll
      for (int ks = 0; ks < XI0; ++ks) { xb[ks] = XLD(ks); if (ks == 4 && XGAP) { SB(); __builtin_amdgcn_s_sleep(XGAP); SB(); } }
      SB();
      const unsigned wa = opaque(L_A + lane * 16), wb = opaque(L_A + 40960 + lane * 16);
      u32x4 wf[3][2];
      ev_wait(ev + C_WQ * 4, 4);
#pragma unroll
      for (int ks = 0; ks < 2; ++ks) { wf[ks][0] = lds_r128(wa + ks * 2048); wf[ks][1] = lds_r128(wa + ks * 2048 + 1024); }
      f32x16 a0 = zero16, a1 = zero16;
      unsigned seen = 0;
      float rstd = 0.f;
      u32x4 cb, ca0, ca1;
#define SQLD(i) ld128(rs, t * 16 + hh * 2048 + (i) * 512, xoff + 40960)
#define SQMAX umax4(umax4(umax4(umax4(0u, sq[0]), sq[1]), sq[2]), sq[3])
#pragma unroll
      for (int g = 0; g < 8; ++g) {
        unsigned mx = 0;
#pragma unroll
        for (int i = 0; i < 5; ++i) mx = umax4(mx, xb[5 * g + i]);
        if (g == 7) mx = umax(mx, SQMAX);
        if (__builtin_expect(unwritten(mx), 0)) {
          unsigned spins = 0;
          do {
            if (give_up(spins)) break;
#pragma unroll
            for (int i = 0; i < 5; ++i) xb[5 * g + i] = XLD(5 * g + i);
            mx = 0;
            if (g == 7) {
#pragma unroll
              for (int i = 0; i < 4; ++i) sq[i] = SQLD(i);
              mx = SQMAX;
            }
#pragma unroll
            for (int i = 0; i < 5; ++i) mx = umax4(mx, xb[5 * g + i]);
          } while (unwritten(mx));
#pragma unroll
          for (int i = 5 * g + 5; i < 40; ++i) if (i < XISS(5 * g)) xb[i] = XLD(i);
        }
        if (g == 0) STAMP(10 + layer * 10);
        if (g == 7) { drain_vm(); ev_post(ev + C_INIT * 4); }
#pragma unroll
        for (int i = 0; i < 5; ++i) {
          const int ks = 5 * g + i, kn = ks + 2, kx = XISS(ks), ky = XISS(ks + 1) - 1;
          if (kn < 40 && kn % 10 == 8) seen = ev_peek(kn < 38 ? ev + (C_WQ + (kn + 2) / 10) * 4 : L_C);
          if (kn < 40 && kn % 10 == 0) ev_wait_from(seen, ev + (C_WQ + kn / 10) * 4, 4);
          if (kn == 32) seen = ev_peek(L_C + m * 256 + C_CS * 4);
          if (kn == 34) {
            ev_wait_from(seen, L_C + m * 256 + C_CS * 4, 4);
            unsigned h0, m0, l0, h1, m1, l1;
            split3(*(LAS float*)(L_M + m * 256 + t * 4), h0, m0, l0);
            split3(*(LAS float*)(L_M + m * 256 + 128 + t * 4), h1, m1, l1);
            ca0 = (u32x4){(h0 >> 16) | h0, (m0 >> 16) | m0, (h0 >> 16) | l0, 0u};
            ca1 = (u32x4){(h1 >> 16) | h1, (m1 >> 16) | m1, (h1 >> 16) | l1, 0u};
          }
          if (kn < 40) {
            const unsigned ad = kn < 20 ? wa + kn * 2048 : wb + (kn - 20) * 2048;
            wf[kn % 3][0] = lds_r128(ad); wf[kn % 3][1] = lds_r128(ad + 1024);
          }
          SB();
          a0 = mfma32(wf[ks % 3][0], xb[ks], a0);
          if (ky > kx && kx < 40) xb[kx] = XLD(kx);
          if (ks == 36) {
            const f32x4 st = (__builtin_bit_cast(f32x4, sq[0]) + __builtin_bit_cast(f32x4, sq[1])) + (__builtin_bit_cast(f32x4, sq[2]) + __builtin_bit_cast(f32x4, sq[3]));
            float sm = st[0] + st[2], ss = st[1] + st[3];
            sm = half_lo(sm) + half_hi(sm);
            ss = half_lo(ss) + half_hi(ss);
            const float mean = sm * (1.f / 640.f);
            rstd = __builtin_amdgcn_rsqf(fmaxf(ss * (1.f / 640.f) - mean * mean, 0.f) + 1e-5f);
            unsigned mh, mm, ml;
            split3(hh ? 0.f : -mean, mh, mm, ml);
            cb = (u32x4){(mh >> 16) | mm, (mh >> 16) | mm, (ml >> 16) | mh, 0u};
          }
          SB();
          a1 = mfma32(wf[ks % 3][1], xb[ks], a1);
          if (ky < 40) xb[ky] = XLD(ky);
          if (ky >= 40 && ky < 44) sq[ky - 40] = SQLD(ky - 40);
          if (ks == 28) ev_post(ev + C_PR * 4);
          SB();
        }
      }
      a1 = mfma32(ca1, cb, a1);
      a0 = mfma32(ca0, cb, a0);
      const unsigned own = m ? y : 4 - y;
      const unsigned tb = L_U + own * UNIT + (tt & 3) * TILE;
      const unsigned vb = tb + T_V + (t >> 3) * VOCT + (t & 7) * 2;
      const unsigned vA = vb + hh * 64, vB = vb + (hh ? 14 : 8) * 16, vC = vb + (hh ? 18 : 12) * 16;
      const float rq = rstd * (0.22360679774997896f * 1.4426950408889634f), r2 = hh ? rstd : rq;
      SB();
      STAMP(2 + layer * 10);
      const unsigned c0 = pack2(a1[8] * rstd, a1[9] * rstd), c1 = pack2(a1[10] * rstd, a1[11] * rstd);
      const unsigned d0 = pack2(a1[12] * rstd, a1[13] * rstd), d1 = pack2(a1[14] * rstd, a1[15] * rstd);
      k1 = (u32x4){pack2(a1[0] * rstd, a1[1] * rstd), pack2(a1[2] * rstd, a1[3] * rstd), pack2(a1[4] * rstd, a1[5] * rstd), pack2(a1[6] * rstd, a1[7] * rstd)};
      k2 = (u32x2){hh ? 0x3F803F80u : c0, hh ? 0x00003F80u : c1};
      lds_w128(tb + lane * 16, k1);
      lds_w64(tb + T_K2 + lane * 8, k2);
      const unsigned cc = hh ? c0 : 0x3F800000u;
      lds_w16lo(vC, cc); lds_w16hi(vC + 16, cc); lds_w16lo(vC + 32, c1); lds_w16hi(vC + 48, c1);
      const unsigned e0 = pack2(a0[8] * r2, a0[9] * r2), e1 = pack2(a0[10] * r2, a0[11] * r2);
      const unsigned f0 = pack2(a0[12] * rstd, a0[13] * rstd), f1 = pack2(a0[14] * rstd, a0[15] * rstd);
      const unsigned b0 = hh ? e0 : d0, b1 = hh ? e1 : d1;
      asm volatile("" ::: "memory");
      lds_w16lo(vB, b0); lds_w16hi(vB + 16, b0); lds_w16lo(vB + 32, b1); lds_w16hi(vB + 48, b1);
      lds_w16lo(vA, f0); lds_w16hi(vA + 16, f0); lds_w16lo(vA + 32, f1); lds_w16hi(vA + 48, f1);
      ev_post(ev + (C_RDY + own) * 4);
      {
        const unsigned go = OFF_KV + layer * KV_LAYER + (h * 64 + c * 8 + tt) * TILE;
        const u32x4 r1 = lds_r128(tb + 1024 + lane * 16), r2 = lds_r128(tb + 2048 + lane * 16);
        st128(k1, rs, lane * 16, go);
        st128(r1, rs, lane * 16, go + 1024);
        st128(r2, rs, lane < 56 ? lane * 16 : OOB, go + 2048);
      }
      q1 = (u32x4){pack2(a0[0] * rq, a0[1] * rq), pack2(a0[2] * rq, a0[3] * rq), pack2(a0[4] * rq, a0[5] * rq), pack2(a0[6] * rq, a0[7] * rq)};
      q2 = (u32x2){hh ? 0u : e0, hh ? 0u : e1};
      if (!PPRI) __builtin_amdgcn_s_setprio(0);
    }
    STAMP(3 + layer * 10);

    {
      LANE_VARS;
      const unsigned kvl = OFF_KV + layer * KV_LAYER + h * 16 * UNIT;
      const unsigned vl = T_V + hh * VOCT + vpos(t) * 16;
      const unsigned lk = L_U + lane * 16, lk2 = L_U + T_K2 + lane * 8, lv = L_U + vl;
      f32x16 o = zero16, sa, sb = zero16;
      Frag f;
      f.vb = (u32x4){0u, 0u, 0u, 0u};
      float mx;
#define TILE_FLAG() do { const u32x2 tg = lds_r64(L_T); drain_vm(); \
        st64(tg, rs, lane == 0 ? 0u : OOB, OFF_KF + ((layer * 32 + h) * 16 + 2 * c + 1 - y) * 128 + (tt & 3) * 8); \
        ev_post(ev + (C_PUB + y) * 4); } while (0)
      if (m == 1) {
        sa = mfma32k8(k2, q2, mfma32(k1, q1, zero16));
        const unsigned qx = L_U + 4 * UNIT - 512 + lane * 8, pe = ev + (w >> 1) * 4;
        lds_w128(qx + lane * 8 + w * 1536, q1);
        if (FPOS == 2) TILE_FLAG();
        SB();
        ev_wait(ev + C_RDY * 4, 4);
        const unsigned po = (w & 1) * 2 * TILE, lkp = lk + po, lk2p = lk2 + po, lvp = lv + po;
        unsigned kb = lkp, k2b = lk2p, vc = lvp, nxt = 0;
        Frag2 g;
        g.k1 = lds_r128(kb);
        g.k2 = lds_r64(k2b);
        g.va = lds_r128(vc);
        g.vb = lds_r128(vc + 2 * VOCT);
        g.vo = (u32x4){0u, 0u, 0u, 0u};
        SB();
        mx = max16(sa);
        mx = fmaxf(half_lo(mx), half_hi(mx));
        set_shift(q2, mx, hh);
        lds_w64(qx + 1024 + w * 1536, q2);
        ev_post(pe + C_QX * 4);
        sa = mfma32k8(g.k2, q2, mfma32(g.k1, q1, zero16));
        if (FPOS == 1) TILE_FLAG();
        f32x16 o2 = zero16;
        ev_wait(pe + C_QX * 4, 2);
        const u32x4 p1 = lds_r128(qx + lane * 8 + (w ^ 1) * 1536);
        const u32x2 p2 = lds_r64(qx + 1024 + (w ^ 1) * 1536);
        ev_post(ev + C_DONE * 4);
        if (FPOS == 0) TILE_FLAG();
        STAMP(4 + layer * 10);
        const unsigned evv = opaque(ev);
#define USTMP(n)
#define UNIT_BODY(PEEK, GATE) \
          USTMP(n); \
          PEEK; \
          att_step2(sa, sb, o, o2, g, q1, q2, p1, p2, kb + TILE, k2b + TILE, vc + TILE); \
          GATE; \
          kb = lkp + nxt; k2b = lk2p + nxt; vc = lvp + nxt; \
          att_step2(sa, sb, o, o2, g, q1, q2, p1, p2, kb, k2b, vc);
        for (int n = 0; n < 8; ++n) {
          nxt += n == 4 ? UNIT - 13 * UNIT : UNIT;
          prio_pat(y, n);
          UNIT_BODY(const unsigned seen = ev_peek(ev + (C_RDY + n + 1) * 4), ev_wait_from<2>(seen, ev + (C_RDY + n + 1) * 4, 4))
        }
        ev_post(ev + (C_DONE + 3) * 4);
        for (int n = 8; n < 16; ++n) {
          nxt += UNIT;
          prio_pat(y, n);
          UNIT_BODY(, unit_gate(n, evv))
        }
        o2 = pv_hi(g.vo, sb, o2);
        __builtin_amdgcn_s_setprio(2);
        const unsigned mg = L_U + 3 * UNIT + lane * 16, m3 = L_U + 3 * UNIT + 16384 + (hh ? 512 + t * 4 : t * 16);
        *(LAS f32x4*)(mg + w * 2048) = (f32x4){o2[0], o2[1], o2[2], o2[3]};
        *(LAS f32x4*)(mg + w * 2048 + 1024) = (f32x4){o2[4], o2[5], o2[6], o2[7]};
        if (hh) *(LAS float*)(m3 + w * 640) = o2[8];
        else *(LAS f32x4*)(m3 + w * 640) = (f32x4){o2[8], o2[9], o2[10], o2[11]};
        ev_post(pe + C_MG * 4);
        ev_wait<0>(pe + C_MG * 4, 2);
        const f32x4 v0 = *(LAS f32x4*)(mg + (w ^ 1) * 2048), v1 = *(LAS f32x4*)(mg + (w ^ 1) * 2048 + 1024);
        const f32x4 v2 = *(LAS f32x4*)(L_U + 3 * UNIT + 16384 + t * 16 + (w ^ 1) * 640);
        const float v3 = *(LAS float*)(L_U + 3 * UNIT + 16384 + 512 + t * 4 + (w ^ 1) * 640);
        SB();
#pragma unroll
        for (int i = 0; i < 4; ++i) { o[i] += v0[i]; o[4 + i] += v1[i]; }
        o[8] += hh ? v3 : v2[0];
        o[9] += v2[1]; o[10] += v2[2]; o[11] += v2[3];
      } else {
        if (APRI == 1) prio_if(y);
        sa = mfma32k8(k2, q2, mfma32(k1, q1, tri_mask<true>()));
        const f32x16 far = tri_mask<false>();
        const unsigned ka = lk + (4 + tt) * TILE, k2a = lk2 + (4 + tt) * TILE, va = lv + (4 + tt) * TILE;
        f.va = lds_r128(va + 8 * TILE);
        const unsigned sn = ev_peek(ev + (C_RDY + 4 - y) * 4);
        SB();
        if (FPOS >= 1) TILE_FLAG();
        mx = max16(sa);
        mx = fmaxf(half_lo(mx), half_hi(mx));
        set_shift(q2, mx, hh);
#pragma unroll
        for (int i = 0; i < 16; ++i) sa[i] -= mx;
        SB();
        if (FPOS == 0) TILE_FLAG();
        ev_wait_from(sn, ev + (C_RDY + 4 - y) * 4, 4);
        const int x1 = tt & 3;
        const unsigned e1 = ev + (C_RDY + 3 - y) * 4, e2 = e1 - 4;
        __builtin_amdgcn_s_setprio(0);
        ev_wait_if_eq(x1, 0, e1, 4);
        STAMP(4 + layer * 10);
        f.k1 = lds_r128(ka + 7 * TILE);
        f.k2 = lds_r64(k2a + 7 * TILE);
#define STEP_W(n, A, B) ev_wait_if_eq(x1, ((n) + 1) & 3, (n) < 3 ? e1 : e2, 4); \
        att_step<1, 1, 1>(A, B, o, f, q1, q2, zero16, va + (8 - (n)) * TILE + 2 * VOCT, ka + (6 - (n)) * TILE, k2a + (6 - (n)) * TILE, va + (7 - (n)) * TILE)
        STEP_W(0, sa, sb);
        STEP_W(1, sb, sa);
        STEP_W(2, sa, sb);
        STEP_W(3, sb, sa);
        STEP_W(4, sa, sb);
        STEP_W(5, sb, sa);
        STEP_W(6, sa, sb);
        att_step<1, 0, 1>(sb, sa, o, f, q1, q2, far, va + TILE + 2 * VOCT, 0, 0, va);
        att_step<0, 0, 0>(sa, sb, o, f, q1, q2, zero16, va + 2 * VOCT, 0, 0, 0);
        o = pv_hi(f.vb, sa, o);
      }
      __builtin_amdgcn_s_setprio(2);
      STAMP(5 + layer * 10);
      const float tot = ((o[0] + o[1]) + (o[2] + o[3])) + ((o[4] + o[5]) + (o[6] + o[7])) + ((o[8] + o[9]) + (o[10] + o[11]));
      const bool bad = !(fabsf(tot) <= 3.0e38f) | !(o[8] <= (hh ? SUM_LIMIT : 3.0e38f));
      if (__builtin_expect(__builtin_amdgcn_ballot_w64(bad) != 0, 0)) {
        const int qt = c * 8 + tt;
        const int lo = m ? 0 : (qt >= 8 ? qt - 8 : 0), hi = m ? 63 : qt;
        ev_wait(ev + C_PUB * 4, 4); ev_wait(ev + (C_PUB + 1) * 4, 4);
        if (hh) { q2.x = 0u; q2.y = 0u; }
        mx = -3.0e38f;
        for (int pass = 0; pass < 2; ++pass) {
          o = zero16;
          for (int kt = lo; kt <= hi; ++kt) {
            const unsigned so = kvl + kt * TILE;
            const u32x4 j1 = ld128(rs, lane * 16, so), v0 = ld128(rs, vl, so), v1 = ld128(rs, vl + 2 * VOCT, so);
            const u32x2 j2 = ld64(rs, lane * 8 + T_K2, so);
            f32x16 cin = zero16;
            if (!m && kt == qt) cin = tri_mask<true>();
            if (!m && kt == qt - 8) cin = tri_mask<false>();
            f32x16 s = mfma32k8(j2, q2, mfma32(j1, q1, cin));
            if (pass == 0) {
              mx = fmaxf(mx, max16(s));
            } else {
#pragma unroll
              for (int e = 0; e < 16; ++e) s[e] = __builtin_amdgcn_exp2f(s[e]);
              o = pv_lo(v0, s, o);
              o = pv_hi(v1, s, o);
            }
          }
          if (pass == 0) { mx = fmaxf(half_lo(mx), half_hi(mx)); set_shift(q2, mx, hh); }
        }
      }
      const float inv = __builtin_amdgcn_rcpf(half_hi(o[8]));
      const u32x4 p = {pack2(o[0] * inv, o[1] * inv), pack2(o[2] * inv, o[3] * inv), pack2(o[4] * inv, o[5] * inv), pack2(o[6] * inv, o[7] * inv)};
      const unsigned oo = OFF_O + layer * XIMG + (c * 16 + tt * 2) * 20480;
      const unsigned lo = (t >> 4) * 20480 + (t & 15) * 16;
      ev_wait_if_eq(layer, 0, L_C + C_SEEN * 4, 1);
      st128(clean4(p), rs, lo + hh * 256, oo + h * 512);
      st64(clean2((u32x2){pack2(o[8] * inv, o[9] * inv), pack2(o[10] * inv, o[11] * inv)}), rs, hh ? OOB : lo, oo + (64 + (h >> 1)) * 256 + (h & 1) * 8);
    }
    STAMP(6 + layer * 10);

    {
      LANE_VARS;
      const int cg = h & 7, t16 = lane & 15, g4 = lane >> 4, kh = (w >> 1) & 1;
      const int rt = c * 16 + ((h >> 3) + 4 - 4 * y) * 2 + (w & 1);
      const int row = rt * 16 + t16;
      const unsigned xl = (row >> 5) * XT + (row & 31) * 16 + cg * 5120;
      const unsigned xo = OFF_X + layer * XIM2;
      const unsigned xa = xl + (4 * kh + g4) * 512, xc = kh ? OOB : xl + (8 + (g4 >> 1)) * 512 + (g4 & 1) * 8;
      const u32x4 ra = ld128(rs, xa, xo);
      const u32x2 rc = ld64(rs, xc, xo);
      const unsigned oo = OFF_O + layer * XIMG + rt * 20480 + kh * 10240;
      const unsigned ol = lane * 16;
#define OLD(k) ld128(rs, ol + ((k) & 3) * 1024, oo + ((k) >> 2) * 4096)
      u32x4 ob[10];
      const unsigned wa0 = opaque(L_A + kh * 51200 + lane * 16);
      constexpr int WD = OWD;
      u32x4 wf[WD + 1];
      if (ODRAIN) drain_vm();
      ev_post(ev + (C_OS + STREAM * y) * 4);
      f32x4 acc[5];
#pragma unroll
      for (int n = 0; n < 5; ++n) acc[n] = (f32x4){0.f, 0.f, 0.f, 0.f};
      ev_wait(ev + (C_OS + STREAM * y) * 4, STREAM ? 4 : 8);
      if (OSLP) __builtin_amdgcn_s_sleep(OSLP);
#pragma unroll
      for (int ks = 0; ks < 10; ++ks) { ob[ks] = OLD(ks); if (ks == 4 && OGAP) { SB(); __builtin_amdgcn_s_sleep(OGAP); SB(); } }
      SB();
      if (OLI) ev_post(ev + C_OL * 4);
      ev_wait(ev + C_WO * 4, 4);
#pragma unroll
      for (int i = 0; i < WD; ++i) wf[i] = lds_r128(wa0 + i * 1024);
#pragma unroll
      for (int g = 0; g < 2; ++g) {
        unsigned mx = 0;
#pragma unroll
        for (int i = 0; i < 5; ++i) mx = umax4(mx, ob[5 * g + i]);
        if (__builtin_expect(unwritten(mx), 0)) {
          unsigned spins = 0;
          do {
            if (give_up(spins)) break;
#pragma unroll
            for (int i = 5 * g; i < 10; ++i) ob[i] = OLD(i);
            mx = 0;
#pragma unroll
            for (int i = 0; i < 5; ++i) mx = umax4(mx, ob[5 * g + i]);
          } while (unwritten(mx));
        }
        if (g == 0) { if (!OLI) ev_post(ev + C_OL * 4); STAMP(7 + layer * 10); }
#pragma unroll
        for (int e = 0; e < 25; ++e) {
          const int i = 25 * g + e, in = i + WD;
          if (in < 50) wf[in % (WD + 1)] = lds_r128(wa0 + in * 1024);
          acc[i % 5] = mfma16(wf[i % (WD + 1)], ob[i / 5], acc[i % 5]);
          SB();
        }
      }
      STAMP(8 + layer * 10);
      const unsigned sc = L_U + (m + y) * UNIT + (w & 1) * 5120 + lane * 16;
      if (OPP == 0) ev_post(ev + C_OP * 4);
      const unsigned pe = ev + (C_RED + y * 2 + (w & 1)) * 4;
      f32x4 u0, u1;
      if (kh) {
        *(LAS f32x4*)(sc) = acc[0]; *(LAS f32x4*)(sc + 1024) = acc[1]; *(LAS f32x4*)(sc + 4096) = acc[4];
        u0 = acc[2]; u1 = acc[3];
      } else {
        *(LAS f32x4*)(sc + 2048) = acc[2]; *(LAS f32x4*)(sc + 3072) = acc[3];
        u0 = acc[0]; u1 = acc[1];
      }
      ev_post(pe);
      u0 += (f32x4){bf_lo(ra.x), bf_hi(ra.x), bf_lo(ra.y), bf_hi(ra.y)};
      u1 += (f32x4){bf_lo(ra.z), bf_hi(ra.z), bf_lo(ra.w), bf_hi(ra.w)};
      f32x4 u2 = acc[4] + (f32x4){bf_lo(rc.x), bf_hi(rc.x), bf_lo(rc.y), bf_hi(rc.y)};
      ev_wait<0>(pe, 2);
      const f32x4 p0 = *(LAS f32x4*)(sc + kh * 2048), p1 = *(LAS f32x4*)(sc + kh * 2048 + 1024), p2 = *(LAS f32x4*)(sc + 4096);
      if (OPP == 1) ev_post(ev + C_OP * 4);
      SB();
      u0 += p0; u1 += p1; u2 += p2;
      const u32x4 na = {pack2(u0[0], u0[1]), pack2(u0[2], u0[3]), pack2(u1[0], u1[1]), pack2(u1[2], u1[3])};
      u32x2 nc = {pack2(u2[0], u2[1]), pack2(u2[2], u2[3])};
      if (layer < 3) {
        SB();
        st128(clean4(na), rs, xa, xo + XIM2);
        st64(clean2(nc), rs, xc, xo + XIM2);
        ev_post(ev + (C_XG + STREAM * y) * 4);
        if (OPP == 2) ev_post(ev + C_OP * 4);
        SB();
        STAMP(50 + layer);
        if (kh) nc = (u32x2){0u, 0u};
        float s0 = sq2(na.x, 0.f), s1 = sum2(na.x, 0.f);
        s0 = sq2(na.y, s0); s1 = sum2(na.y, s1); s0 = sq2(na.z, s0); s1 = sum2(na.z, s1); s0 = sq2(na.w, s0); s1 = sum2(na.w, s1);
        s0 = sq2(nc.x, s0); s1 = sum2(nc.x, s1); s0 = sq2(nc.y, s0); s1 = sum2(nc.y, s1);
        s0 = half_lo(s0) + half_hi(s0);
        s1 = half_lo(s1) + half_hi(s1);
        const auto q0 = __builtin_amdgcn_permlane16_swap(__float_as_uint(s0), __float_as_uint(s0), false, false);
        const auto q1 = __builtin_amdgcn_permlane16_swap(__float_as_uint(s1), __float_as_uint(s1), false, false);
        s0 = __uint_as_float(q0[0]) + __uint_as_float(q0[1]);
        s1 = __uint_as_float(q1[0]) + __uint_as_float(q1[1]);
        st64(clean2((u32x2){__float_as_uint(s1), __float_as_uint(s0)}), rs,
             lane < 16 ? (row >> 5) * XT + 40960 + (row & 31) * 16 + cg * 512 + kh * 8 : OOB, xo + XIM2);
      } else {
        const unsigned od = row * 1280 + cg * 160;
        st128a<0>(na, ro, od + kh * 64 + g4 * 16, 0);
        __builtin_amdgcn_raw_buffer_store_b64(nc, ro, kh ? OOB : od + 128 + g4 * 8, 0, 0);
      }
      if (layer < 3) { ev_wait(ev + (C_XG + STREAM * y) * 4, STREAM ? 4 : 8); if (XSLP) __builtin_amdgcn_s_sleep(XSLP); }
    }
    STAMP(9 + layer * 10);
  }
  STAMP_OUT();
}

extern "C" void kernel_launch(void* const* d_in, const int* in_sizes, int n_in, void* d_out, int out_size, void* d_ws, size_t ws_size,
                              hipStream_t stream) {
  (void)in_sizes; (void)n_in; (void)out_size; (void)ws_size;
  (void)hipFuncSetAttribute((const void*)main_kernel, hipFuncAttributeMaxDynamicSharedMemorySize, LDS_BYTES);
  main_kernel<<<256, 768, LDS_BYTES, stream>>>((const u16*)d_in[0], (const u16*)d_in[1], (const u16*)d_in[2], (const u16*)d_in[4],
                                               (const u16*)d_in[5], (char*)d_ws, (u16*)d_out);
}
```

```cpp
#include <hip/hip_runtime.h>
#include <hip/hip_bf16.h>

typedef float f32x16 __attribute__((ext_vector_type(16)));
typedef float f32x8 __attribute__((ext_vector_type(8)));
typedef float f32x4 __attribute__((ext_vector_type(4)));
typedef unsigned u32x4 __attribute__((ext_vector_type(4)));
typedef unsigned u32x2 __attribute__((ext_vector_type(2)));
typedef __bf16 bf16x16 __attribute__((ext_vector_type(16)));
typedef __bf16 bf16x8 __attribute__((ext_vector_type(8)));
typedef __bf16 bf16x2 __attribute__((ext_vector_type(2)));
typedef short s16x4 __attribute__((ext_vector_type(4)));
typedef unsigned short u16;

#define LAS __attribute__((address_space(3)))
typedef unsigned long long u64;
#define DEVI __device__ __forceinline__

constexpr unsigned WQ_HEAD = 81920;
constexpr unsigned WO_CG = 102400;
constexpr unsigned XIMG = 2621440;
constexpr unsigned XT = 45056, XIM2 = 64 * XT;
constexpr unsigned VOCT = 352;
constexpr unsigned TILE = 2944, T_K2 = 1024, T_V = 1536;
constexpr unsigned UNIT = 4 * TILE;
constexpr unsigned KV_LAYER = 32 * 64 * TILE;
constexpr unsigned OFF_FLAG = 0;
constexpr unsigned OFF_F1 = 4096, OFF_F2 = OFF_F1 + 32768;
constexpr unsigned OFF_KF = OFF_F2 + 32768;
constexpr unsigned OFF_WQ = OFF_KF + 4 * 32 * 16 * 128;
constexpr unsigned OFF_WO = OFF_WQ + 2 * 32 * WQ_HEAD;
constexpr unsigned OFF_X = OFF_WO + 2 * 8 * WO_CG;
constexpr unsigned OFF_O = OFF_X + 4 * XIM2;
constexpr unsigned OFF_KV = OFF_O + 4 * XIMG;
constexpr unsigned WS_USED = OFF_KV + 4 * KV_LAYER;
constexpr unsigned SENT = 0xFFFFFFFFu;
constexpr unsigned OOB = 0xE0000000u;

constexpr unsigned L_A = 0;
constexpr unsigned L_U = 102400;
constexpr unsigned L_M = L_U + 5 * UNIT;
constexpr unsigned L_C = L_M + 512;
constexpr unsigned L_T = L_C + 1024;
constexpr unsigned LDS_BYTES = 163840;
enum {
  C_RDY = 0, C_DONE = 16,
  C_WO = 32, C_OP = 33, C_WQ = 34,
  C_RED = 38, C_MG = 42, C_QX = 60,
  C_PR = 47, C_PUB = 53, C_OL = 55, C_OS = 56,
  C_WF = 46, C_W0 = 48, C_X0 = 49, C_INIT = 50, C_SEEN = 51, C_CS = 52, C_F1 = 59
};

constexpr unsigned kSpinCap = 1u << 17;
constexpr float SUM_LIMIT = 1.0e18f;
constexpr int XI0 = 8, XD = 12;
constexpr int OWD = 16;
constexpr int WOE = 40;
constexpr int PMASK = 0x6DB6;

DEVI unsigned pack2(float a, float b) {
  bf16x2 v = {(__bf16)a, (__bf16)b};
  return __builtin_bit_cast(unsigned, v);
}
DEVI float sq2(unsigned u, float acc) {
  const bf16x2 p = __builtin_bit_cast(bf16x2, u);
  return __builtin_amdgcn_fdot2_f32_bf16(p, p, acc, false);
}
DEVI float bf_lo(unsigned u) { return __uint_as_float(u << 16); }
DEVI float bf_hi(unsigned u) { return __uint_as_float(u & 0xffff0000u); }
DEVI f32x16 mfma32(u32x4 a, u32x4 b, f32x16 c) {
  return __builtin_amdgcn_mfma_f32_32x32x16_bf16(__builtin_bit_cast(bf16x8, a), __builtin_bit_cast(bf16x8, b), c, 0, 0, 0);
}
DEVI f32x16 mfma32k8(u32x2 a, u32x2 b, f32x16 c) {
  return __builtin_amdgcn_mfma_f32_32x32x8bf16_1k(__builtin_bit_cast(s16x4, a), __builtin_bit_cast(s16x4, b), c, 0, 0, 0);
}
DEVI f32x4 mfma16(u32x4 a, u32x4 b, f32x4 c) {
  return __builtin_amdgcn_mfma_f32_16x16x32_bf16(__builtin_bit_cast(bf16x8, a), __builtin_bit_cast(bf16x8, b), c, 0, 0, 0);
}
DEVI f32x16 pv_lo(u32x4 v, const f32x16& p, f32x16 o) {
  f32x8 h = __builtin_shufflevector(p, p, 0, 1, 2, 3, 4, 5, 6, 7);
  return __builtin_amdgcn_smfmac_f32_32x32x32_bf16(__builtin_bit_cast(bf16x8, v), __builtin_bit_cast(bf16x16, h), o, 0xDDDD, 0, 0);
}
DEVI f32x16 pv_hi(u32x4 v, const f32x16& p, f32x16 o) {
  f32x8 h = __builtin_shufflevector(p, p, 8, 9, 10, 11, 12, 13, 14, 15);
  return __builtin_amdgcn_smfmac_f32_32x32x32_bf16(__builtin_bit_cast(bf16x8, v), __builtin_bit_cast(bf16x16, h), o, 0xDDDD, 0, 0);
}
DEVI u32x4 lds_r128(unsigned a) { return *(LAS u32x4*)a; }
DEVI u32x2 lds_r64(unsigned a) { return *(LAS u32x2*)a; }
DEVI void lds_w128(unsigned a, u32x4 v) { *(LAS u32x4*)a = v; }
DEVI void lds_w64(unsigned a, u32x2 v) { *(LAS u32x2*)a = v; }
DEVI float half_lo(float v) { return __uint_as_float(__builtin_amdgcn_permlane32_swap(__float_as_uint(v), __float_as_uint(v), false, false)[0]); }
DEVI float half_hi(float v) { return __uint_as_float(__builtin_amdgcn_permlane32_swap(__float_as_uint(v), __float_as_uint(v), false, false)[1]); }
DEVI unsigned umax(unsigned a, unsigned b) { return a > b ? a : b; }
DEVI unsigned umax4(unsigned m, u32x4 v) { return umax(umax(umax(umax(m, v.x), v.y), v.z), v.w); }
DEVI bool unwritten(unsigned m) { return __builtin_amdgcn_ballot_w64(m == SENT) != 0; }
DEVI unsigned clean(unsigned v) { return v < SENT ? v : SENT - 1; }
DEVI u32x4 clean4(u32x4 v) { return (u32x4){clean(v.x), clean(v.y), clean(v.z), clean(v.w)}; }
DEVI u32x2 clean2(u32x2 v) { return (u32x2){clean(v.x), clean(v.y)}; }
DEVI unsigned vpos(int d) {
  const unsigned q = ((14u | 0u << 5 | 4u << 10 | 18u << 15 | 8u << 20) >> ((d >> 2) * 5 & 31)) & 31u;
  return d < 20 ? q + (d & 3) : (d == 20 ? 13u : 12u);
}
DEVI void lds_w16lo(unsigned a, unsigned v) { *(LAS u16*)a = (u16)v; }
DEVI void lds_w16hi(unsigned a, unsigned v) { *(LAS u16*)a = (u16)(v >> 16); }
DEVI float sum2(unsigned u, float acc) {
  const bf16x2 ones = {(__bf16)1.0f, (__bf16)1.0f};
  return __builtin_amdgcn_fdot2_f32_bf16(__builtin_bit_cast(bf16x2, u), ones, acc, false);
}
DEVI void split3(float v, unsigned& hi, unsigned& mid, unsigned& lo) {
  hi = __float_as_uint(v) & 0xffff0000u;
  const float r1 = v - __uint_as_float(hi);
  mid = __float_as_uint(r1) & 0xffff0000u;
  lo = __float_as_uint(r1 - __uint_as_float(mid)) & 0xffff0000u;
}
#define SB() __builtin_amdgcn_sched_barrier(0)
DEVI void bar_lds() {
  asm volatile("s_waitcnt lgkmcnt(0)\n\ts_barrier" ::: "memory");
}
DEVI void drain_vm() { asm volatile("s_waitcnt vmcnt(0)" ::: "memory"); }
#define DMA_ASM(d, v, r, o) "s_mov_b32 m0, " d "\n\ts_nop 3\n\tbuffer_load_dwordx4 " v ", " r ", " o " offen sc1 lds\n\t"
DEVI void dma1k(unsigned lds_dst, unsigned voff, u32x4 srd, unsigned soff) {
  unsigned keep;
  asm volatile("s_mov_b32 %0, m0\n\t" DMA_ASM("%1", "%2", "%3", "%4") "s_mov_b32 m0, %0"
               : "=&s"(keep) : "s"(__builtin_amdgcn_readfirstlane(lds_dst)), "v"(voff), "s"(srd), "s"(__builtin_amdgcn_readfirstlane(soff)) : "memory");
}
typedef __amdgpu_buffer_rsrc_t rsrc_t;
DEVI u32x4 ld128(rsrc_t r, unsigned voff, unsigned soff) { return __builtin_amdgcn_raw_buffer_load_b128(r, voff, soff, 16); }
DEVI u32x2 ld64(rsrc_t r, unsigned voff, unsigned soff) { return __builtin_amdgcn_raw_buffer_load_b64(r, voff, soff, 16); }
template <int AUX> DEVI void st128a(u32x4 v, rsrc_t r, unsigned voff, unsigned soff) {
  __builtin_amdgcn_raw_buffer_store_b128(v, r, voff, soff, AUX);
  asm volatile("s_nop 1" :: "v"(v) : "memory");
}
DEVI void st128(u32x4 v, rsrc_t r, unsigned voff, unsigned soff) { st128a<16>(v, r, voff, soff); }
DEVI void st64(u32x2 v, rsrc_t r, unsigned voff, unsigned soff) { __builtin_amdgcn_raw_buffer_store_b64(v, r, voff, soff, 16); }

DEVI bool give_up(unsigned& spins) {
  asm volatile("s_sleep 2" ::: "memory");
  return ++spins > kSpinCap;
}
DEVI u32x2 next_tag(u32x2 g) {
  u64 v = ((u64)g.y << 32 | g.x) + 1;
  v += v == ~0ull;
  return (u32x2){(unsigned)v, (unsigned)(v >> 32)};
}
DEVI void wait_tag(rsrc_t rs, unsigned off, u32x2 tag) {
  for (unsigned spins = 0;;) {
    const u32x2 v = ld64(rs, off, 0);
    if (__builtin_amdgcn_ballot_w64(off != OOB && (v.x != tag.x || v.y != tag.y)) == 0 || give_up(spins)) break;
  }
  asm volatile("" ::: "memory");
}

DEVI void transpose8x4(const u32x2 (&r)[8], u32x4 (&o)[4]) {
#pragma unroll
  for (int p = 0; p < 4; ++p) {
    o[0][p] = __builtin_amdgcn_perm(r[2 * p + 1].x, r[2 * p].x, 0x05040100u);
    o[1][p] = __builtin_amdgcn_perm(r[2 * p + 1].x, r[2 * p].x, 0x07060302u);
    o[2][p] = __builtin_amdgcn_perm(r[2 * p + 1].y, r[2 * p].y, 0x05040100u);
    o[3][p] = __builtin_amdgcn_perm(r[2 * p + 1].y, r[2 * p].y, 0x07060302u);
  }
}

DEVI void wq_item_load(u32x2 (&r)[8], rsrc_t rw, int h, int kh, int qd, bool on) {
  const int mt = qd >> 3, rq = qd & 7;
  const int col = h * 20 + (rq < 5 ? mt * 640 + 4 * rq : 1280 + mt * 12 + 4 * (rq - 5));
  const unsigned off = (on && qd < 15) ? kh * 8 * 3840 + col * 2 : OOB;
#pragma unroll
  for (int j = 0; j < 8; ++j) r[j] = __builtin_amdgcn_raw_buffer_load_b64(rw, off, j * 3840, 0);
}
DEVI unsigned wq_item_pos(int kh, int qd) { return (kh >> 1) * 2048 + (qd >> 3) * 1024 + (kh & 1) * 512 + (qd & 7) * 64; }

struct Frag { u32x4 k1; u32x2 k2; u32x4 va, vb; };

template <bool QK, bool LDK, bool LDV>
DEVI void att_step(f32x16& cur, f32x16& oth, f32x16& o, Frag& f, const u32x4& q1, const u32x2& q2, const f32x16& cin,
                   unsigned a_vb, unsigned a_k1, unsigned a_k2, unsigned a_va) {
  o = pv_hi(f.vb, oth, o);
  f.vb = lds_r128(a_vb);
  SB();
#pragma unroll
  for (int i = 0; i < 4; ++i) cur[i] = __builtin_amdgcn_exp2f(cur[i]);
  SB();
  if (QK) oth = mfma32(f.k1, q1, cin);
  if (LDK) f.k1 = lds_r128(a_k1);
  SB();
#pragma unroll
  for (int i = 4; i < 8; ++i) cur[i] = __builtin_amdgcn_exp2f(cur[i]);
  SB();
  if (QK) oth = mfma32k8(f.k2, q2, oth);
  if (LDK) f.k2 = lds_r64(a_k2);
  SB();
#pragma unroll
  for (int i = 8; i < 12; ++i) cur[i] = __builtin_amdgcn_exp2f(cur[i]);
  SB();
  o = pv_lo(f.va, cur, o);
  if (LDV) f.va = lds_r128(a_va);
  SB();
#pragma unroll
  for (int i = 12; i < 16; ++i) cur[i] = __builtin_amdgcn_exp2f(cur[i]);
  SB();
}
struct Frag2 { u32x4 k1; u32x2 k2; u32x4 va, vb, vo; };
#define EXP4(s, b) do { SB(); _Pragma("unroll") for (int i_ = b; i_ < b + 4; ++i_) s[i_] = __builtin_amdgcn_exp2f(s[i_]); SB(); } while (0)
DEVI void att_step2(f32x16& sa, f32x16& sb, f32x16& oa, f32x16& ob, Frag2& f, const u32x4& qa1, const u32x2& qa2,
                    const u32x4& qb1, const u32x2& qb2, unsigned a_k1, unsigned a_k2, unsigned a_v) {
  const f32x16 z = {0.f, 0.f, 0.f, 0.f, 0.f, 0.f, 0.f, 0.f, 0.f, 0.f, 0.f, 0.f, 0.f, 0.f, 0.f, 0.f};
  ob = pv_hi(f.vo, sb, ob);
  EXP4(sa, 0);
  sb = mfma32(f.k1, qb1, z);
  f.k1 = lds_r128(a_k1);
  EXP4(sa, 4);
  sb = mfma32k8(f.k2, qb2, sb);
  f.k2 = lds_r64(a_k2);
  EXP4(sa, 8);
  oa = pv_lo(f.va, sa, oa);
  EXP4(sa, 12);
  oa = pv_hi(f.vb, sa, oa);
  f.vo = f.vb;
  EXP4(sb, 0);
  sa = mfma32(f.k1, qa1, z);
  f.vb = lds_r128(a_v + 2 * VOCT);
  EXP4(sb, 4);
  sa = mfma32k8(f.k2, qa2, sa);
  EXP4(sb, 8);
  ob = pv_lo(f.va, sb, ob);
  f.va = lds_r128(a_v);
  EXP4(sb, 12);
}
DEVI void prio_pat(int y, int n) {
  asm volatile("s_setprio 0\n\ts_bitcmp1_b32 %0, %1\n\ts_cbranch_scc0 .Lskip%=\n\ts_setprio 1\n.Lskip%=:" :: "s"(y ? PMASK : 0), "s"(n) : "scc");
}
DEVI int opaque(int v) { asm volatile("" : "+v"(v)); return v; }

DEVI float max16(const f32x16& s) {
  float m = fmaxf(fmaxf(s[0], s[1]), s[2]);
#pragma unroll
  for (int i = 3; i < 15; i += 2) m = fmaxf(fmaxf(m, s[i]), s[i + 1]);
  return fmaxf(m, s[15]);
}
DEVI void set_shift(u32x2& q2, float m, int hh) {
  unsigned hi, mid, lo;
  split3(-m, hi, mid, lo);
  q2.x = hh ? (hi >> 16) | mid : q2.x;
  q2.y = hh ? (lo >> 16) | 0xFF7F0000u : q2.y;
}
template <bool DIAG> DEVI f32x16 tri_mask() {
  f32x16 c;
#pragma unroll
  for (int i = 0; i < 16; ++i) {
    const int key = (i & 3) + 8 * (i >> 2);
    const u64 above = ((1ull << key) - 1) | ((1ull << (key + 4)) - 1) << 32;
    c[i] = __builtin_amdgcn_inverse_ballot_w64(DIAG ? above : ~above) ? -3.0e38f : 0.f;
  }
  return c;
}

DEVI void ev_wait_if_eq(int a, int b, unsigned addr, unsigned target) {
  unsigned sv, cnt, v;
  asm volatile("s_cmp_lg_u32 %3, %4\n\ts_cbranch_scc1 .Lgo%=\n\ts_mov_b32 %1, 0x400000\n\ts_nop 7\n\ts_nop 3\n"
               ".Lspin%=:\n\tds_read_b32 %2, %5\n\ts_waitcnt lgkmcnt(0)\n\tv_readfirstlane_b32 %0, %2\n\t"
               "s_cmp_ge_u32 %0, %6\n\ts_cbranch_scc1 .Lgo%=\n\ts_sleep 1\n\ts_sub_u32 %1, %1, 1\n\ts_cbranch_scc0 .Lspin%=\n"
               ".Lgo%=:"
               : "=&s"(sv), "=&s"(cnt), "=&v"(v) : "s"(a), "s"(b), "v"(addr), "s"(target) : "memory", "scc");
}
DEVI void unit_gate(int n, unsigned evv) {
  unsigned sv, cnt, v, a;
  asm volatile("s_bitcmp1_b32 0x3200, %4\n\ts_cbranch_scc0 .Lgo%=\n\ts_nop 7\n\ts_nop 3\n\t"
               "v_lshl_add_u32 %3, %4, 2, %5\n\tv_mov_b32 %2, 1\n\ts_mov_b64 exec, 1\n\tds_add_u32 %3, %2 offset:%6\n\ts_mov_b64 exec, -1\n\t"
               "s_mov_b32 %1, 0x400000\n"
               ".Lspin%=:\n\tds_read_b32 %2, %3 offset:%7\n\ts_waitcnt lgkmcnt(0)\n\tv_readfirstlane_b32 %0, %2\n\t"
               "s_cmp_ge_u32 %0, 4\n\ts_cbranch_scc1 .Lgo%=\n\ts_sleep 1\n\ts_sub_u32 %1, %1, 1\n\ts_cbranch_scc0 .Lspin%=\n"
               ".Lgo%=:"
               : "=&s"(sv), "=&s"(cnt), "=&v"(v), "=&v"(a) : "s"(n), "v"(evv), "i"(C_DONE * 4), "i"(C_RDY * 4 + 4) : "memory", "scc");
}
DEVI void ev_post(unsigned addr, unsigned n = 1) {
  asm volatile("s_mov_b64 exec, 1\n\tds_add_u32 %0, %1\n\ts_mov_b64 exec, -1" :: "v"(addr), "v"(n) : "memory");
}
DEVI unsigned ev_peek(unsigned addr) { return *(volatile LAS unsigned*)addr; }
template <int NAP = 1> DEVI void ev_wait_from(unsigned seen, unsigned addr, unsigned target) {
  unsigned sv, cnt;
  asm volatile("s_nop 0\n\tv_readfirstlane_b32 %0, %2\n\ts_cmp_ge_u32 %0, %4\n\ts_cbranch_scc1 .Lgo%=\n\t"
               "s_mov_b32 %1, 0x400000\n"
               ".Lspin%=:\n\ts_sleep %5\n\tds_read_b32 %2, %3\n\ts_sub_u32 %1, %1, 1\n\ts_cbranch_scc1 .Lout%=\n\t"
               "s_waitcnt lgkmcnt(0)\n\tv_readfirstlane_b32 %0, %2\n\t"
               "s_cmp_ge_u32 %0, %4\n\ts_cbranch_scc0 .Lspin%=\n"
               ".Lout%=:\n\ts_waitcnt lgkmcnt(0)\n"
               ".Lgo%=:"
               : "=&s"(sv), "=&s"(cnt), "+&v"(seen)
               : "v"(addr), "s"(__builtin_amdgcn_readfirstlane(target)), "i"(NAP) : "memory", "scc");
}
template <int NAP = 1> DEVI void ev_wait(unsigned addr, unsigned target) { ev_wait_from<NAP>(ev_peek(addr), addr, target); }

DEVI unsigned unit_home(int n) { return L_U + (n < 5 ? n : n - 13) * UNIT; }
DEVI void kv_fetch(unsigned l16, u32x4 srd, unsigned un, unsigned dst, int first, int step) {
  for (int i = first; i < 12; i += step) {
    const unsigned o = i < 11 ? i * 1024 : UNIT - 1024;
    dma1k(dst + o, l16, srd, OFF_KV + un * UNIT + o);
  }
}
DEVI int kv_order(int c, int j) { return c < 4 ? (j < 4 ? (c - j) & 3 : 4 + ((c - j) & 3)) : (j < 5 ? (c - j) & 3 : 4 + ((c - j) & 3)); }
DEVI void kv_fake(unsigned l0, unsigned l5, unsigned slot) {
#pragma unroll
  for (int i = 0; i < 6; ++i) {
    const unsigned o = (l0 + i * 2048) % TILE, one = (o >= 1280 && o < T_V) ? 0x3F800000u : 0u;
    if (i < 5 || l5 != OOB) lds_w128(L_U + slot * UNIT + l0 + i * 2048, (u32x4){0u, one, 0u, one});
  }
}
DEVI unsigned o_piece(unsigned q, int l, int c, int h) { return OFF_O + l * XIMG + c * 327680 + h * 10240 + q * 1024; }
DEVI unsigned x_piece(unsigned q, int l, int c, int h) { return OFF_X + l * XIM2 + c * 8 * XT + h * (XT / 4) + q * 1024; }
DEVI void add_colsum(const u32x4 (&wn)[20], int u, int lane, unsigned dst) {
  float s0 = 0.f, s1 = 0.f, s2 = 0.f, s3 = 0.f;
#pragma unroll
  for (int i = 0; i < 20; ++i) {
    s0 += bf_lo(wn[i].x); s1 += bf_hi(wn[i].x); s2 += bf_lo(wn[i].y); s3 += bf_hi(wn[i].y);
    s0 += bf_lo(wn[i].z); s1 += bf_hi(wn[i].z); s2 += bf_lo(wn[i].w); s3 += bf_hi(wn[i].w);
  }
  s0 = (s0 + s1) + (s2 + s3);
  s0 = half_lo(s0) + half_hi(s0);
  if (lane < 32) __hip_atomic_fetch_add((LAS float*)(dst + (u & 1) * 128 + lane * 4), s0, __ATOMIC_RELAXED, __HIP_MEMORY_SCOPE_WORKGROUP);
}

DEVI void weight_share(int task, int lane, int c, int h, const u16* wq, const u16* wo, rsrc_t rs, unsigned bounce, bool bar) {
  const int m2 = task >> 2, wi = task & 3;
  u32x2 r[8];
  u32x4 o[4];
  unsigned dst, pos, np;
  if (wi < 3) {
    const rsrc_t rw = __builtin_amdgcn_make_buffer_rsrc((void*)wq, 0, 640 * 3840, 0x00020000);
    wq_item_load(r, rw, h, c * 10 + wi * 4 + (lane >> 4), lane & 15, wi < 2 || lane < 32);
    pos = wq_item_pos(lane >> 4, lane & 15);
    dst = OFF_WQ + (m2 * 32 + h) * WQ_HEAD + (c * 10 + wi * 4) * 1024;
    np = wi < 2 ? 4 : 2;
  } else {
    const int b32 = (h >> 3) * 8 + c, p = b32 * 4 + (lane >> 4), nt = p % 5, q = (p / 5) * 4 + ((lane >> 2) & 3), g = lane & 3;
    const int c4 = nt < 4 ? (nt >> 1) * 8 + g * 2 + (nt & 1) : 16 + g;
    const rsrc_t rw = __builtin_amdgcn_make_buffer_rsrc((void*)wo, 0, 640 * 1280, 0x00020000);
    const int k0 = q < 64 ? (q >> 1) * 20 + (q & 1) * 4 : (q - 64) * 40 + 16, dk = q < 64 ? 8 : 20;
    const unsigned wof = b32 < 25 ? k0 * 1280 + ((h & 7) * 80 + c4 * 4) * 2 : OOB;
#pragma unroll
    for (int j = 0; j < 4; ++j) {
      r[j] = __builtin_amdgcn_raw_buffer_load_b64(rw, wof, j * 1280, 0);
      r[4 + j] = __builtin_amdgcn_raw_buffer_load_b64(rw, b32 < 25 ? wof + dk * 1280 : OOB, j * 1280, 0);
    }
    pos = lane * 64;
    dst = OFF_WO + (m2 * 8 + (h & 7)) * WO_CG + b32 * 4096;
    np = b32 < 25 ? 4 : 0;
  }
  if (bar) bar_lds();
  transpose8x4(r, o);
#pragma unroll
  for (int i = 0; i < 4; ++i) if (pos < np * 1024) lds_w128(bounce + pos + i * 16, o[i]);
#pragma unroll
  for (int i = 0; i < 4; ++i) o[i] = lds_r128(bounce + i * 1024 + lane * 16);
#pragma unroll
  for (int i = 0; i < 4; ++i) st128(o[i], rs, (unsigned)i < np ? lane * 16 : OOB, dst + i * 1024);
}

__global__ void __launch_bounds__(768, 3) main_kernel(const u16* __restrict__ x, const u16* __restrict__ wqA, const u16* __restrict__ woA,
                                                      const u16* __restrict__ wqB, const u16* __restrict__ woB,
                                                      char* __restrict__ ws, u16* __restrict__ out) {
  const int tid0 = threadIdx.x;
  const int w = __builtin_amdgcn_readfirstlane(tid0 >> 6);
  const int y = (w >> 2) & 1;
  const int tt = y ? 7 - w : w + 4;
  const int xg = blockIdx.x & 7, xi = blockIdx.x >> 3;
  const int c = ((xi & 1) << 2) | ((xg & 1) << 1) | ((xi >> 1) & 1);
  const int h = ((xg >> 1) << 3) | (xi >> 2);
#define LANE_VARS const int tid = opaque(tid0), lane = tid & 63, t = lane & 31, hh = lane >> 5; (void)t; (void)hh
  const rsrc_t rs = __builtin_amdgcn_make_buffer_rsrc(ws, 0, WS_USED, 0x00020000);
  const rsrc_t ro = __builtin_amdgcn_make_buffer_rsrc(out, 0, XIMG, 0x00020000);
  const u32x4 srd = {(unsigned)(size_t)ws, (unsigned)((size_t)ws >> 32) & 0xffffu, WS_USED, 0x00020000u};
  const f32x16 zero16 = {0.f, 0.f, 0.f, 0.f, 0.f, 0.f, 0.f, 0.f, 0.f, 0.f, 0.f, 0.f, 0.f, 0.f, 0.f, 0.f};
  if (tid0 < 384) *(LAS unsigned*)(L_M + tid0 * 4) = 0u;

  if (w >= 8) {
    const int u = w - 8, lane = tid0 & 63, me = h * 8 + c;
    const unsigned l16 = lane * 16, l4 = u * 1024 + l16;
    const unsigned l0 = (u & 1) * 1024 + l16, l5 = l0 + 10240 < UNIT ? l0 : OOB;
    const u32x4 sv = {SENT, SENT, SENT, SENT};
    __builtin_amdgcn_s_setprio(1);
    u32x2 tag = ld64(rs, 0, OFF_FLAG);
    {
      const rsrc_t rx = __builtin_amdgcn_make_buffer_rsrc((void*)x, 0, XIMG, 0x00020000);
      const int row = c * 256 + h * 8 + (lane & 7), p = lane >> 3;
      u32x4 v[3];
#pragma unroll
      for (int i = 0; i < 3; ++i) v[i] = __builtin_amdgcn_raw_buffer_load_b128(rx, (i < 2 || u < 2) ? row * 1280 + p * 16 : OOB, (u + 4 * i) * 128, 0);
      if (u == 2) st128(sv, rs, lane < 16 ? OFF_F1 + p * 32768 + me * 128 + (lane & 7) * 16 : OOB, 0);
      if (u == 3) st128(sv, rs, OFF_KF + (((p >> 1) * 32 + h) * 16 + 2 * c + (p & 1)) * 128 + (lane & 7) * 16, 0);
      bar_lds();
      const unsigned xd = OFF_X + (row >> 5) * XT;
#pragma unroll
      for (int i = 0; i < 3; ++i) st128(clean4(v[i]), rs, (i < 2 || u < 2) ? xd + p * 512 + (row & 31) * 16 : OOB, (u + 4 * i) * 4096);
      float ss = 0.f, sm = 0.f;
#pragma unroll
      for (int i = 0; i < 3; ++i) {
        ss = sq2(v[i].x, ss); ss = sq2(v[i].y, ss); ss = sq2(v[i].z, ss); ss = sq2(v[i].w, ss);
        sm = sum2(v[i].x, sm); sm = sum2(v[i].y, sm); sm = sum2(v[i].z, sm); sm = sum2(v[i].w, sm);
      }
      ss = half_lo(ss) + half_hi(ss);
      sm = half_lo(sm) + half_hi(sm);
      st64(clean2((u32x2){__float_as_uint(sm), __float_as_uint(ss)}), rs,
           lane < 32 ? xd + 40960 + (row & 31) * 16 + (u * 2 + (p >> 1)) * 512 + (p & 1) * 8 : OOB, 0);
      tag = next_tag(tag);
      if (u == 0) lds_w64(L_T, tag);
      drain_vm();
      ev_post(L_C + C_W0 * 4);
      if (u == 0) {
        ev_wait(L_C + C_W0 * 4, 7);
        st64(tag, rs, lane == 0 ? 0u : OOB, OFF_F1 + me * 128);
        ev_post(L_C + C_F1 * 4);
        wait_tag(rs, lane < 8 ? OFF_F1 + (h * 8 + lane) * 128 : OOB, tag);
        ev_post(L_C + C_WF * 4);
      } else {
        ev_wait(L_C + C_WF * 4, 1);
      }
    }
    {
      u32x4 wn[20];
#pragma unroll
      for (int i = 0; i < 20; ++i) wn[i] = ld128(rs, l4, OFF_WQ + h * WQ_HEAD + i * 4096);
#pragma unroll
      for (int q = 0; q < 4; ++q) {
#pragma unroll
        for (int i = 5 * q; i < 5 * q + 5; ++i) lds_w128(L_A + l4 + i * 4096, wn[i]);
        ev_post(L_C + (C_WQ + q) * 4);
      }
      add_colsum(wn, u, lane, L_M);
      ev_post(L_C + C_CS * 4);
      if (u) {
        for (unsigned q = 0; q < 10u + (u == 3); ++q) {
          st128(sv, rs, l16, u < 3 ? o_piece(q, u - 1, c, h) : x_piece(q, 1, c, h));
          __builtin_amdgcn_s_sleep(2);
        }
      }
      drain_vm();
      ev_post(L_C + C_INIT * 4);
      if (u == 3) {
        ev_wait<4>(L_C + C_INIT * 4, 12);
        st64(tag, rs, lane == 0 ? 0u : OOB, OFF_F2 + me * 128);
      }
    }
    for (int layer = 0; layer < 4; ++layer) {
      const int m = layer & 1;
      const unsigned ev = L_C + layer * 256;
      const unsigned ub = (layer * 32 + h) * 16, wo_src = OFF_WO + (m * 8 + (h & 7)) * WO_CG;
      const unsigned hi = u < 2;
      const unsigned wq_next = layer < 3 ? OFF_WQ + ((m ^ 1) * 32 + h) * WQ_HEAD : OOB;
      ev_wait<8>(ev + C_PR * 4, 1);
      if (!hi && layer == 2) {
        ev_wait<2>(ev + C_PR * 4, 8);
        for (int p = u - 2; p < WOE; p += 2) dma1k(L_A + p * 1024, l16, srd, wo_src + p * 1024);
      }
      if (layer == 0 && u == 3) {
        ev_wait<2>(ev + C_PR * 4, 8);
        wait_tag(rs, OFF_F2 + (lane < 32 ? lane * 8 + c : (((lane >> 3) & 3) * 8 + (h & 7)) * 8 + (lane & 7)) * 128, tag);
        ev_post(L_C + C_SEEN * 4);
      }
      ev_wait(ev + (C_PUB + 1 - hi) * 4, 4);
      {
        const unsigned un = ub + 2 * (m ? kv_order(c, 1) : (c - 1) & 7) + hi, slot = m ? 3 - hi : 1 + hi;
        ev_wait(ev + C_PR * 4, 8);
        if (m || c > 0) {
          wait_tag(rs, lane < 4 ? OFF_KF + un * 128 + lane * 8 : OOB, tag);
          if (m && !hi) ev_wait(ev + C_DONE * 4, 8);
          kv_fetch(l16, srd, un, L_U + slot * UNIT, u & 1, 2);
          drain_vm();
        } else {
          kv_fake(l0, l5, slot);
        }
        ev_post(ev + (C_RDY + slot) * 4, 2);
      }
      if (m == 0) {
        if (layer == 0) {
          ev_wait(L_C + C_SEEN * 4, 1);
          ev_wait(ev + (C_RDY + 3) * 4, 4); ev_wait(ev + (C_RDY + 4) * 4, 4);
          for (int p = hi ? u : 68 + u; p < (hi ? 70 : 100); p += 2) dma1k(L_A + p * 1024, l16, srd, wo_src + p * 1024);
        } else if (hi) {
          ev_wait(ev + (C_RDY + 3) * 4, 4);
          for (int p = WOE + u; p < 100; p += 2) dma1k(L_A + p * 1024, l16, srd, wo_src + p * 1024);
        }
      } else {
        ev_wait(ev + C_PUB * 4, 4); ev_wait(ev + (C_PUB + 1) * 4, 4);
        ev_post(ev + (C_RDY + 10) * 4); ev_post(ev + (C_RDY + 14) * 4);
        for (int n = 4 + u; n < 17; n += 4) {
          if (n >= 13 && layer == 1) {
            for (unsigned q = 0; q < 10u + (u >> 1); ++q) {
              st128(sv, rs, l16, u < 2 ? o_piece(q, 2 + u, c, h) : x_piece(q, u, c, h));
              __builtin_amdgcn_s_sleep(2);
            }
          }
          if (n < 16) {
            const unsigned un2 = ub + 2 * kv_order(c, n >> 1) + 1 - (n & 1);
            wait_tag(rs, lane < 4 ? OFF_KF + un2 * 128 + lane * 8 : OOB, tag);
            if (n == 4) ev_wait(ev + C_DONE * 4, 8);
            if (n >= 13) ev_wait<8>(ev + (C_DONE + 3) * 4, 8);
            kv_fetch(l16, srd, un2, unit_home(n), 0, 1);
          }
          drain_vm();
          if (n != 8) ev_post(ev + (C_RDY + (n < 8 ? n : n < 13 ? 8 : 13)) * 4, n < 8 ? 4 : 1);
        }
        if (layer == 3) {
          ev_wait<4>(ev + (C_RDY + 13) * 4, 4);
          st64(tag, rs, (tid0 == 512 && blockIdx.x == 0) ? 0u : OOB, OFF_FLAG);
        }
      }
      if (m == 0) {
        drain_vm();
        ev_post(ev + C_WO * 4);
        ev_wait<4>(ev + C_OS * 4, 8);
        ev_wait<2>(ev + C_OL * 4, 4);
      }
      u32x4 wn[20];
#pragma unroll
      for (int i = 0; i < 20; ++i) wn[i] = ld128(rs, l4, wq_next + i * 4096);
      SB();
      if (m) {
        ev_wait<16>(ev + (C_DONE + 9) * 4, 8);
        for (int p = u; p < 52; p += 4) { dma1k(L_A + p * 1024, l16, srd, wo_src + p * 1024); __builtin_amdgcn_s_sleep(1); }
        ev_wait<8>(ev + (C_DONE + 13) * 4, 8);
        for (int p = 52 + u; p < 100; p += 4) dma1k(L_A + p * 1024, l16, srd, wo_src + p * 1024);
        drain_vm();
        ev_post(ev + C_WO * 4);
        if (layer == 3) break;
      }
      if (m) { ev_wait<8>(ev + C_OS * 4, 1); ev_wait<4>(ev + C_OL * 4, 4); }
      ev_wait(ev + C_OP * 4, 8);
#pragma unroll
      for (int q = 0; q < 4; ++q) {
#pragma unroll
        for (int i = 5 * q; i < 5 * q + 5; ++i) lds_w128(L_A + l4 + i * 4096, wn[i]);
        ev_post(ev + 256 + (C_WQ + q) * 4);
      }
      if (layer == 0) { add_colsum(wn, u, lane, L_M + 256); ev_post(ev + 256 + C_CS * 4); }
    }
    return;
  }

  {
    const int lane = tid0 & 63;
    if (w < 3) {
      weight_share(w, lane, c, h, wqA, woA, rs, L_U + w * 4096, true);
      drain_vm();
      ev_post(L_C + C_W0 * 4);
    } else {
      const unsigned id = (blockIdx.x * 5 + w - 3) * 64 + lane;
      const rsrc_t r1 = __builtin_amdgcn_make_buffer_rsrc((void*)wqB, 0, 640 * 3840, 0x00020000);
      const rsrc_t r2 = __builtin_amdgcn_make_buffer_rsrc((void*)woA, 0, 640 * 1280, 0x00020000);
      const rsrc_t r3 = __builtin_amdgcn_make_buffer_rsrc((void*)woB, 0, 640 * 1280, 0x00020000);
      bar_lds();
      __builtin_amdgcn_s_sleep(30);
      {
        const u32x4 p0 = __builtin_amdgcn_raw_buffer_load_b128(r1, id * 16, 0, 0), p1 = __builtin_amdgcn_raw_buffer_load_b128(r1, id * 16, 1310720, 0);
        const u32x4 p2 = __builtin_amdgcn_raw_buffer_load_b128(r2, id * 16, 0, 0), p3 = __builtin_amdgcn_raw_buffer_load_b128(r3, id * 16, 0, 0);
        asm volatile("" :: "v"(p0), "v"(p1), "v"(p2), "v"(p3));
      }
      ev_wait(L_C + C_F1 * 4, 1);
      weight_share(w, lane, c, h, wqB, w == 3 ? woA : woB, rs, L_U + w * 4096, false);
    }
  }

  for (int layer = 0; layer < 4; ++layer) {
    const int m = layer & 1;
    const unsigned ev = L_C + layer * 256;
    u32x4 q1, k1; u32x2 q2, k2;
    {
      LANE_VARS;
      __builtin_amdgcn_s_setprio(1);
      const unsigned xoff = OFF_X + layer * XIM2 + (c * 8 + tt) * XT;
      const unsigned xl = lane * 16;
#define XLD(k) ld128(rs, xl + ((k) & 3) * 1024, xoff + ((k) >> 2) * 4096)
#define XISS(ks) (XI0 + 2 * (ks) < XD + (ks) ? XI0 + 2 * (ks) : XD + (ks))
      u32x4 xb[40];
      u32x4 sq[4];
      if (layer == 0) {
        if (w == 0) {
          ev_wait(L_C + C_F1 * 4, 1);
          wait_tag(rs, lane < 32 ? OFF_F1 + (lane * 8 + c) * 128 : OOB, lds_r64(L_T));
          ev_post(L_C + C_X0 * 4);
        }
        ev_wait(L_C + C_X0 * 4, 1);
        ev_wait(L_C + C_WF * 4, 1);
        drain_vm();
        ev_post(L_C + C_INIT * 4);
      }
#pragma unroll
      for (int ks = 0; ks < XI0; ++ks) xb[ks] = XLD(ks);
      SB();
      const unsigned wa = opaque(L_A + lane * 16), wb = opaque(L_A + 40960 + lane * 16);
      u32x4 wf[3][2];
      ev_wait(ev + C_WQ * 4, 4);
#pragma unroll
      for (int ks = 0; ks < 2; ++ks) { wf[ks][0] = lds_r128(wa + ks * 2048); wf[ks][1] = lds_r128(wa + ks * 2048 + 1024); }
      f32x16 a0 = zero16, a1 = zero16;
      unsigned seen = 0;
      float rstd = 0.f;
      u32x4 cb, ca0, ca1;
#define SQLD(i) ld128(rs, t * 16 + hh * 2048 + (i) * 512, xoff + 40960)
#define SQMAX umax4(umax4(umax4(umax4(0u, sq[0]), sq[1]), sq[2]), sq[3])
#pragma unroll
      for (int g = 0; g < 8; ++g) {
        unsigned mx = 0;
#pragma unroll
        for (int i = 0; i < 5; ++i) mx = umax4(mx, xb[5 * g + i]);
        if (g == 7) mx = umax(mx, SQMAX);
        if (__builtin_expect(unwritten(mx), 0)) {
          unsigned spins = 0;
          do {
            if (give_up(spins)) break;
#pragma unroll
            for (int i = 0; i < 5; ++i) xb[5 * g + i] = XLD(5 * g + i);
            mx = 0;
            if (g == 7) {
#pragma unroll
              for (int i = 0; i < 4; ++i) sq[i] = SQLD(i);
              mx = SQMAX;
            }
#pragma unroll
            for (int i = 0; i < 5; ++i) mx = umax4(mx, xb[5 * g + i]);
          } while (unwritten(mx));
#pragma unroll
          for (int i = 5 * g + 5; i < 40; ++i) if (i < XISS(5 * g)) xb[i] = XLD(i);
        }
#pragma unroll
        for (int i = 0; i < 5; ++i) {
          const int ks = 5 * g + i, kn = ks + 2, kx = XISS(ks), ky = XISS(ks + 1) - 1;
          if (kn < 40 && kn % 10 == 8) seen = ev_peek(kn < 38 ? ev + (C_WQ + (kn + 2) / 10) * 4 : L_C);
          if (kn < 40 && kn % 10 == 0) ev_wait_from(seen, ev + (C_WQ + kn / 10) * 4, 4);
          if (kn == 32) seen = ev_peek(L_C + m * 256 + C_CS * 4);
          if (kn == 34) {
            ev_wait_from(seen, L_C + m * 256 + C_CS * 4, 4);
            unsigned h0, m0, l0, h1, m1, l1;
            split3(*(LAS float*)(L_M + m * 256 + t * 4), h0, m0, l0);
            split3(*(LAS float*)(L_M + m * 256 + 128 + t * 4), h1, m1, l1);
            ca0 = (u32x4){(h0 >> 16) | h0, (m0 >> 16) | m0, (h0 >> 16) | l0, 0u};
            ca1 = (u32x4){(h1 >> 16) | h1, (m1 >> 16) | m1, (h1 >> 16) | l1, 0u};
          }
          if (kn < 40) {
            const unsigned ad = kn < 20 ? wa + kn * 2048 : wb + (kn - 20) * 2048;
            wf[kn % 3][0] = lds_r128(ad); wf[kn % 3][1] = lds_r128(ad + 1024);
          }
          SB();
          a0 = mfma32(wf[ks % 3][0], xb[ks], a0);
          if (ky > kx && kx < 40) xb[kx] = XLD(kx);
          if (ks == 36) {
            const f32x4 st = (__builtin_bit_cast(f32x4, sq[0]) + __builtin_bit_cast(f32x4, sq[1])) + (__builtin_bit_cast(f32x4, sq[2]) + __builtin_bit_cast(f32x4, sq[3]));
            float sm = st[0] + st[2], ss = st[1] + st[3];
            sm = half_lo(sm) + half_hi(sm);
            ss = half_lo(ss) + half_hi(ss);
            const float mean = sm * (1.f / 640.f);
            rstd = __builtin_amdgcn_rsqf(fmaxf(ss * (1.f / 640.f) - mean * mean, 0.f) + 1e-5f);
            unsigned mh, mm, ml;
            split3(hh ? 0.f : -mean, mh, mm, ml);
            cb = (u32x4){(mh >> 16) | mm, (mh >> 16) | mm, (ml >> 16) | mh, 0u};
          }
          SB();
          a1 = mfma32(wf[ks % 3][1], xb[ks], a1);
          if (ky < 40) xb[ky] = XLD(ky);
          if (ky >= 40 && ky < 44) sq[ky - 40] = SQLD(ky - 40);
          if (ks == 28) ev_post(ev + C_PR * 4);
          SB();
        }
      }
      a1 = mfma32(ca1, cb, a1);
      a0 = mfma32(ca0, cb, a0);
      const unsigned own = m ? y : 4 - y;
      const unsigned tb = L_U + own * UNIT + (tt & 3) * TILE;
      const unsigned vb = tb + T_V + (t >> 3) * VOCT + (t & 7) * 2;
      const unsigned vA = vb + hh * 64, vB = vb + (hh ? 14 : 8) * 16, vC = vb + (hh ? 18 : 12) * 16;
      const float rq = rstd * (0.22360679774997896f * 1.4426950408889634f), r2 = hh ? rstd : rq;
      SB();
      const unsigned c0 = pack2(a1[8] * rstd, a1[9] * rstd), c1 = pack2(a1[10] * rstd, a1[11] * rstd);
      const unsigned d0 = pack2(a1[12] * rstd, a1[13] * rstd), d1 = pack2(a1[14] * rstd, a1[15] * rstd);
      k1 = (u32x4){pack2(a1[0] * rstd, a1[1] * rstd), pack2(a1[2] * rstd, a1[3] * rstd), pack2(a1[4] * rstd, a1[5] * rstd), pack2(a1[6] * rstd, a1[7] * rstd)};
      k2 = (u32x2){hh ? 0x3F803F80u : c0, hh ? 0x00003F80u : c1};
      lds_w128(tb + lane * 16, k1);
      lds_w64(tb + T_K2 + lane * 8, k2);
      const unsigned cc = hh ? c0 : 0x3F800000u;
      lds_w16lo(vC, cc); lds_w16hi(vC + 16, cc); lds_w16lo(vC + 32, c1); lds_w16hi(vC + 48, c1);
      const unsigned e0 = pack2(a0[8] * r2, a0[9] * r2), e1 = pack2(a0[10] * r2, a0[11] * r2);
      const unsigned f0 = pack2(a0[12] * rstd, a0[13] * rstd), f1 = pack2(a0[14] * rstd, a0[15] * rstd);
      const unsigned b0 = hh ? e0 : d0, b1 = hh ? e1 : d1;
      asm volatile("" ::: "memory");
      lds_w16lo(vB, b0); lds_w16hi(vB + 16, b0); lds_w16lo(vB + 32, b1); lds_w16hi(vB + 48, b1);
      lds_w16lo(vA, f0); lds_w16hi(vA + 16, f0); lds_w16lo(vA + 32, f1); lds_w16hi(vA + 48, f1);
      ev_post(ev + (C_RDY + own) * 4);
      {
        const unsigned go = OFF_KV + layer * KV_LAYER + (h * 64 + c * 8 + tt) * TILE;
        const u32x4 r1 = lds_r128(tb + 1024 + lane * 16), r2 = lds_r128(tb + 2048 + lane * 16);
        st128(k1, rs, lane * 16, go);
        st128(r1, rs, lane * 16, go + 1024);
        st128(r2, rs, lane < 56 ? lane * 16 : OOB, go + 2048);
      }
      q1 = (u32x4){pack2(a0[0] * rq, a0[1] * rq), pack2(a0[2] * rq, a0[3] * rq), pack2(a0[4] * rq, a0[5] * rq), pack2(a0[6] * rq, a0[7] * rq)};
      q2 = (u32x2){hh ? 0u : e0, hh ? 0u : e1};
    }

    {
      LANE_VARS;
      const unsigned kvl = OFF_KV + layer * KV_LAYER + h * 16 * UNIT;
      const unsigned vl = T_V + hh * VOCT + vpos(t) * 16;
      const unsigned lk = L_U + lane * 16, lk2 = L_U + T_K2 + lane * 8, lv = L_U + vl;
      f32x16 o = zero16, sa, sb = zero16;
      Frag f;
      f.vb = (u32x4){0u, 0u, 0u, 0u};
      float mx;
#define TILE_FLAG() do { const u32x2 tg = lds_r64(L_T); drain_vm(); \
        st64(tg, rs, lane == 0 ? 0u : OOB, OFF_KF + ((layer * 32 + h) * 16 + 2 * c + 1 - y) * 128 + (tt & 3) * 8); \
        ev_post(ev + (C_PUB + y) * 4); } while (0)
      if (m == 1) {
        sa = mfma32k8(k2, q2, mfma32(k1, q1, zero16));
        const unsigned qx = L_U + 4 * UNIT - 512 + lane * 8, pe = ev + (w >> 1) * 4;
        lds_w128(qx + lane * 8 + w * 1536, q1);
        SB();
        ev_wait(ev + C_RDY * 4, 4);
        const unsigned po = (w & 1) * 2 * TILE, lkp = lk + po, lk2p = lk2 + po, lvp = lv + po;
        unsigned kb = lkp, k2b = lk2p, vc = lvp, nxt = 0;
        Frag2 g;
        g.k1 = lds_r128(kb);
        g.k2 = lds_r64(k2b);
        g.va = lds_r128(vc);
        g.vb = lds_r128(vc + 2 * VOCT);
        g.vo = (u32x4){0u, 0u, 0u, 0u};
        SB();
        mx = max16(sa);
        mx = fmaxf(half_lo(mx), half_hi(mx));
        set_shift(q2, mx, hh);
        lds_w64(qx + 1024 + w * 1536, q2);
        ev_post(pe + C_QX * 4);
        sa = mfma32k8(g.k2, q2, mfma32(g.k1, q1, zero16));
        TILE_FLAG();
        f32x16 o2 = zero16;
        ev_wait(pe + C_QX * 4, 2);
        const u32x4 p1 = lds_r128(qx + lane * 8 + (w ^ 1) * 1536);
        const u32x2 p2 = lds_r64(qx + 1024 + (w ^ 1) * 1536);
        ev_post(ev + C_DONE * 4);
        const unsigned evv = opaque(ev);
#define UNIT_BODY(PEEK, GATE) \
          PEEK; \
          att_step2(sa, sb, o, o2, g, q1, q2, p1, p2, kb + TILE, k2b + TILE, vc + TILE); \
          GATE; \
          kb = lkp + nxt; k2b = lk2p + nxt; vc = lvp + nxt; \
          att_step2(sa, sb, o, o2, g, q1, q2, p1, p2, kb, k2b, vc);
        for (int n = 0; n < 8; ++n) {
          nxt += n == 4 ? UNIT - 13 * UNIT : UNIT;
          prio_pat(y, n);
          UNIT_BODY(const unsigned seen = ev_peek(ev + (C_RDY + n + 1) * 4), ev_wait_from<2>(seen, ev + (C_RDY + n + 1) * 4, 4))
        }
        ev_post(ev + (C_DONE + 3) * 4);
        for (int n = 8; n < 16; ++n) {
          nxt += UNIT;
          prio_pat(y, n);
          UNIT_BODY(, unit_gate(n, evv))
        }
        o2 = pv_hi(g.vo, sb, o2);
        __builtin_amdgcn_s_setprio(2);
        const unsigned mg = L_U + 3 * UNIT + lane * 16, m3 = L_U + 3 * UNIT + 16384 + (hh ? 512 + t * 4 : t * 16);
        *(LAS f32x4*)(mg + w * 2048) = (f32x4){o2[0], o2[1], o2[2], o2[3]};
        *(LAS f32x4*)(mg + w * 2048 + 1024) = (f32x4){o2[4], o2[5], o2[6], o2[7]};
        if (hh) *(LAS float*)(m3 + w * 640) = o2[8];
        else *(LAS f32x4*)(m3 + w * 640) = (f32x4){o2[8], o2[9], o2[10], o2[11]};
        ev_post(pe + C_MG * 4);
        ev_wait<0>(pe + C_MG * 4, 2);
        const f32x4 v0 = *(LAS f32x4*)(mg + (w ^ 1) * 2048), v1 = *(LAS f32x4*)(mg + (w ^ 1) * 2048 + 1024);
        const f32x4 v2 = *(LAS f32x4*)(L_U + 3 * UNIT + 16384 + t * 16 + (w ^ 1) * 640);
        const float v3 = *(LAS float*)(L_U + 3 * UNIT + 16384 + 512 + t * 4 + (w ^ 1) * 640);
        SB();
#pragma unroll
        for (int i = 0; i < 4; ++i) { o[i] += v0[i]; o[4 + i] += v1[i]; }
        o[8] += hh ? v3 : v2[0];
        o[9] += v2[1]; o[10] += v2[2]; o[11] += v2[3];
      } else {
        sa = mfma32k8(k2, q2, mfma32(k1, q1, tri_mask<true>()));
        const f32x16 far = tri_mask<false>();
        const unsigned ka = lk + (4 + tt) * TILE, k2a = lk2 + (4 + tt) * TILE, va = lv + (4 + tt) * TILE;
        f.va = lds_r128(va + 8 * TILE);
        const unsigned sn = ev_peek(ev + (C_RDY + 4 - y) * 4);
        SB();
        TILE_FLAG();
        mx = max16(sa);
        mx = fmaxf(half_lo(mx), half_hi(mx));
        set_shift(q2, mx, hh);
#pragma unroll
        for (int i = 0; i < 16; ++i) sa[i] -= mx;
        SB();
        ev_wait_from(sn, ev + (C_RDY + 4 - y) * 4, 4);
        const int x1 = tt & 3;
        const unsigned e1 = ev + (C_RDY + 3 - y) * 4, e2 = e1 - 4;
        __builtin_amdgcn_s_setprio(0);
        ev_wait_if_eq(x1, 0, e1, 4);
        f.k1 = lds_r128(ka + 7 * TILE);
        f.k2 = lds_r64(k2a + 7 * TILE);
#define STEP_W(n, A, B) ev_wait_if_eq(x1, ((n) + 1) & 3, (n) < 3 ? e1 : e2, 4); \
        att_step<1, 1, 1>(A, B, o, f, q1, q2, zero16, va + (8 - (n)) * TILE + 2 * VOCT, ka + (6 - (n)) * TILE, k2a + (6 - (n)) * TILE, va + (7 - (n)) * TILE)
        STEP_W(0, sa, sb);
        STEP_W(1, sb, sa);
        STEP_W(2, sa, sb);
        STEP_W(3, sb, sa);
        STEP_W(4, sa, sb);
        STEP_W(5, sb, sa);
        STEP_W(6, sa, sb);
        att_step<1, 0, 1>(sb, sa, o, f, q1, q2, far, va + TILE + 2 * VOCT, 0, 0, va);
        att_step<0, 0, 0>(sa, sb, o, f, q1, q2, zero16, va + 2 * VOCT, 0, 0, 0);
        o = pv_hi(f.vb, sa, o);
      }
      __builtin_amdgcn_s_setprio(2);
      const float tot = ((o[0] + o[1]) + (o[2] + o[3])) + ((o[4] + o[5]) + (o[6] + o[7])) + ((o[8] + o[9]) + (o[10] + o[11]));
      const bool bad = !(fabsf(tot) <= 3.0e38f) | !(o[8] <= (hh ? SUM_LIMIT : 3.0e38f));
      if (__builtin_expect(__builtin_amdgcn_ballot_w64(bad) != 0, 0)) {
        const int qt = c * 8 + tt;
        const int lo = m ? 0 : (qt >= 8 ? qt - 8 : 0), hi = m ? 63 : qt;
        ev_wait(ev + C_PUB * 4, 4); ev_wait(ev + (C_PUB + 1) * 4, 4);
        if (hh) { q2.x = 0u; q2.y = 0u; }
        mx = -3.0e38f;
        for (int pass = 0; pass < 2; ++pass) {
          o = zero16;
          for (int kt = lo; kt <= hi; ++kt) {
            const unsigned so = kvl + kt * TILE;
            const u32x4 j1 = ld128(rs, lane * 16, so), v0 = ld128(rs, vl, so), v1 = ld128(rs, vl + 2 * VOCT, so);
            const u32x2 j2 = ld64(rs, lane * 8 + T_K2, so);
            f32x16 cin = zero16;
            if (!m && kt == qt) cin = tri_mask<true>();
            if (!m && kt == qt - 8) cin = tri_mask<false>();
            f32x16 s = mfma32k8(j2, q2, mfma32(j1, q1, cin));
            if (pass == 0) {
              mx = fmaxf(mx, max16(s));
            } else {
#pragma unroll
              for (int e = 0; e < 16; ++e) s[e] = __builtin_amdgcn_exp2f(s[e]);
              o = pv_lo(v0, s, o);
              o = pv_hi(v1, s, o);
            }
          }
          if (pass == 0) { mx = fmaxf(half_lo(mx), half_hi(mx)); set_shift(q2, mx, hh); }
        }
      }
      const float inv = __builtin_amdgcn_rcpf(half_hi(o[8]));
      const u32x4 p = {pack2(o[0] * inv, o[1] * inv), pack2(o[2] * inv, o[3] * inv), pack2(o[4] * inv, o[5] * inv), pack2(o[6] * inv, o[7] * inv)};
      const unsigned oo = OFF_O + layer * XIMG + (c * 16 + tt * 2) * 20480;
      const unsigned lo = (t >> 4) * 20480 + (t & 15) * 16;
      ev_wait_if_eq(layer, 0, L_C + C_SEEN * 4, 1);
      st128(clean4(p), rs, lo + hh * 256, oo + h * 512);
      st64(clean2((u32x2){pack2(o[8] * inv, o[9] * inv), pack2(o[10] * inv, o[11] * inv)}), rs, hh ? OOB : lo, oo + (64 + (h >> 1)) * 256 + (h & 1) * 8);
    }

    {
      LANE_VARS;
      const int cg = h & 7, t16 = lane & 15, g4 = lane >> 4, kh = (w >> 1) & 1;
      const int rt = c * 16 + ((h >> 3) + 4 - 4 * y) * 2 + (w & 1);
      const int row = rt * 16 + t16;
      const unsigned xl = (row >> 5) * XT + (row & 31) * 16 + cg * 5120;
      const unsigned xo = OFF_X + layer * XIM2;
      const unsigned xa = xl + (4 * kh + g4) * 512, xc = kh ? OOB : xl + (8 + (g4 >> 1)) * 512 + (g4 & 1) * 8;
      const u32x4 ra = ld128(rs, xa, xo);
      const u32x2 rc = ld64(rs, xc, xo);
      const unsigned oo = OFF_O + layer * XIMG + rt * 20480 + kh * 10240;
      const unsigned ol = lane * 16;
#define OLD(k) ld128(rs, ol + ((k) & 3) * 1024, oo + ((k) >> 2) * 4096)
      u32x4 ob[10];
      const unsigned wa0 = opaque(L_A + kh * 51200 + lane * 16);
      constexpr int WD = OWD;
      u32x4 wf[WD + 1];
      ev_post(ev + C_OS * 4);
      f32x4 acc[5];
#pragma unroll
      for (int n = 0; n < 5; ++n) acc[n] = (f32x4){0.f, 0.f, 0.f, 0.f};
      ev_wait(ev + C_OS * 4, 8);
      __builtin_amdgcn_s_sleep(6);
#pragma unroll
      for (int ks = 0; ks < 10; ++ks) ob[ks] = OLD(ks);
      SB();
      ev_wait(ev + C_WO * 4, 4);
#pragma unroll
      for (int i = 0; i < WD; ++i) wf[i] = lds_r128(wa0 + i * 1024);
#pragma unroll
      for (int g = 0; g < 2; ++g) {
        unsigned mx = 0;
#pragma unroll
        for (int i = 0; i < 5; ++i) mx = umax4(mx, ob[5 * g + i]);
        if (__builtin_expect(unwritten(mx), 0)) {
          unsigned spins = 0;
          do {
            if (give_up(spins)) break;
#pragma unroll
            for (int i = 5 * g; i < 10; ++i) ob[i] = OLD(i);
            mx = 0;
#pragma unroll
            for (int i = 0; i < 5; ++i) mx = umax4(mx, ob[5 * g + i]);
          } while (unwritten(mx));
        }
        if (g == 0) ev_post(ev + C_OL * 4);
#pragma unroll
        for (int e = 0; e < 25; ++e) {
          const int i = 25 * g + e, in = i + WD;
          if (in < 50) wf[in % (WD + 1)] = lds_r128(wa0 + in * 1024);
          acc[i % 5] = mfma16(wf[i % (WD + 1)], ob[i / 5], acc[i % 5]);
          SB();
        }
      }
      const unsigned sc = L_U + (m + y) * UNIT + (w & 1) * 5120 + lane * 16;
      const unsigned pe = ev + (C_RED + y * 2 + (w & 1)) * 4;
      f32x4 u0, u1;
      if (kh) {
        *(LAS f32x4*)(sc) = acc[0]; *(LAS f32x4*)(sc + 1024) = acc[1]; *(LAS f32x4*)(sc + 4096) = acc[4];
        u0 = acc[2]; u1 = acc[3];
      } else {
        *(LAS f32x4*)(sc + 2048) = acc[2]; *(LAS f32x4*)(sc + 3072) = acc[3];
        u0 = acc[0]; u1 = acc[1];
      }
      ev_post(pe);
      u0 += (f32x4){bf_lo(ra.x), bf_hi(ra.x), bf_lo(ra.y), bf_hi(ra.y)};
      u1 += (f32x4){bf_lo(ra.z), bf_hi(ra.z), bf_lo(ra.w), bf_hi(ra.w)};
      f32x4 u2 = acc[4] + (f32x4){bf_lo(rc.x), bf_hi(rc.x), bf_lo(rc.y), bf_hi(rc.y)};
      ev_wait<0>(pe, 2);
      const f32x4 p0 = *(LAS f32x4*)(sc + kh * 2048), p1 = *(LAS f32x4*)(sc + kh * 2048 + 1024), p2 = *(LAS f32x4*)(sc + 4096);
      SB();
      u0 += p0; u1 += p1; u2 += p2;
      const u32x4 na = {pack2(u0[0], u0[1]), pack2(u0[2], u0[3]), pack2(u1[0], u1[1]), pack2(u1[2], u1[3])};
      u32x2 nc = {pack2(u2[0], u2[1]), pack2(u2[2], u2[3])};
      if (layer < 3) {
        SB();
        st128(clean4(na), rs, xa, xo + XIM2);
        st64(clean2(nc), rs, xc, xo + XIM2);
        ev_post(ev + C_OP * 4);
        SB();
        if (kh) nc = (u32x2){0u, 0u};
        float s0 = sq2(na.x, 0.f), s1 = sum2(na.x, 0.f);
        s0 = sq2(na.y, s0); s1 = sum2(na.y, s1); s0 = sq2(na.z, s0); s1 = sum2(na.z, s1); s0 = sq2(na.w, s0); s1 = sum2(na.w, s1);
        s0 = sq2(nc.x, s0); s1 = sum2(nc.x, s1); s0 = sq2(nc.y, s0); s1 = sum2(nc.y, s1);
        s0 = half_lo(s0) + half_hi(s0);
        s1 = half_lo(s1) + half_hi(s1);
        const auto q0 = __builtin_amdgcn_permlane16_swap(__float_as_uint(s0), __float_as_uint(s0), false, false);
        const auto q1 = __builtin_amdgcn_permlane16_swap(__float_as_uint(s1), __float_as_uint(s1), false, false);
        s0 = __uint_as_float(q0[0]) + __uint_as_float(q0[1]);
        s1 = __uint_as_float(q1[0]) + __uint_as_float(q1[1]);
        st64(clean2((u32x2){__float_as_uint(s1), __float_as_uint(s0)}), rs,
             lane < 16 ? (row >> 5) * XT + 40960 + (row & 31) * 16 + cg * 512 + kh * 8 : OOB, xo + XIM2);
      } else {
        const unsigned od = row * 1280 + cg * 160;
        st128a<0>(na, ro, od + kh * 64 + g4 * 16, 0);
        __builtin_amdgcn_raw_buffer_store_b64(nc, ro, kh ? OOB : od + 128 + g4 * 8, 0, 0);
      }
      if (layer < 3) { ev_wait(ev + C_OP * 4, 8); __builtin_amdgcn_s_sleep(4); }
    }
  }
}

extern "C" void kernel_launch(void* const* d_in, const int* in_sizes, int n_in, void* d_out, int out_size, void* d_ws, size_t ws_size,
                              hipStream_t stream) {
  (void)in_sizes; (void)n_in; (void)out_size; (void)ws_size;
  (void)hipFuncSetAttribute((const void*)main_kernel, hipFuncAttributeMaxDynamicSharedMemorySize, LDS_BYTES);
  main_kernel<<<256, 768, LDS_BYTES, stream>>>((const u16*)d_in[0], (const u16*)d_in[1], (const u16*)d_in[2], (const u16*)d_in[4],
                                               (const u16*)d_in[5], (char*)d_ws, (u16*)d_out);
}
```
